```python
import torch

def _softmax(x, axis=-1):
    return torch.softmax(x, dim=axis)

def _gelu(x):
    return torch.nn.functional.gelu(x, approximate='tanh')

def _silu(x):
    return torch.nn.functional.silu(x)

def _sigmoid(x):
    return torch.sigmoid(x)

def _layernorm(x, axis=-1, eps=1e-05):
    m = x.mean(dim=axis, keepdim=True)
    v = x.var(dim=axis, keepdim=True, unbiased=False)
    return (x - m) / torch.sqrt(v + eps)

def _scatter_add(tgt, idx, src, axis=0):
    return tgt.clone().index_add_(axis, idx, src)

def _segment_sum(src, seg, n):
    out = torch.zeros((n,) + src.shape[1:], dtype=src.dtype)
    return out.index_add_(0, seg, src)

def _pad(x, pw):
    flat = tuple((v for pair in reversed(pw) for v in pair))
    return torch.nn.functional.pad(x, flat)
kernel_name = 'adaLN_cond__mha_block_cached__glu_gated_glu__decode'

def setup_inputs():

    _w = lambda a: (lambda t: t.to(torch.bfloat16) if t.is_floating_point() else t)(torch.as_tensor(a))

    def _A_setup():
        B, Sq, D = (2, 1, 448)
        H, Sk, Dh = (16, 8192, 256)
        return {'x': _w((lambda *s: torch.randn(*s, dtype=torch.bfloat16) * 0.05)(B, Sq, D)), 'k_cache': _w((lambda *s: torch.randn(*s, dtype=torch.bfloat16) * 3.2)(B, H, Sk, Dh)), 'v_cache': _w((lambda *s: torch.randn(*s, dtype=torch.bfloat16) * 0.4)(B, H, Sk, Dh)), 'wq': _w((lambda *s: torch.randn(*s, dtype=torch.bfloat16) * 3.2)(D, H * Dh) * D ** (-0.5)), 'wo': _w((lambda *s: torch.randn(*s, dtype=torch.bfloat16) * 0.4)(H * Dh, D) * (H * Dh) ** (-0.5))}
    _A_in = _A_setup()

    def _B_setup():
        B, S, D, F = (2, 1, 448, 1792)
        return {'x': _w((lambda *s: torch.randn(*s, dtype=torch.bfloat16) * 0.05)(B, S, D)), 'w_gate': _w((lambda *s: torch.randn(*s, dtype=torch.bfloat16) * 0.05)(D, F)), 'w_up': _w((lambda *s: torch.randn(*s, dtype=torch.bfloat16) * 0.05)(D, F)), 'w_down': _w((lambda *s: torch.randn(*s, dtype=torch.bfloat16) * 0.05)(F, D))}
    _B_in = _B_setup()
    return {'A_x': _A_in['x'], 'A_k_cache': _A_in['k_cache'], 'A_v_cache': _A_in['v_cache'], 'A_wq': _A_in['wq'], 'A_wo': _A_in['wo'], 'B_x': _B_in['x'], 'B_w_gate': _B_in['w_gate'], 'B_w_up': _B_in['w_up'], 'B_w_down': _B_in['w_down'], 'c': _w((lambda *s: torch.randn(*s, dtype=torch.bfloat16) * 0.1)(2, 448) * 8.0), 'w_mod': _w((lambda *s: torch.randn(*s, dtype=torch.bfloat16) * 0.1)(448, 6 * 448) * 448 ** (-0.5) * 8.0)}

def reference(A_x, A_k_cache, A_v_cache, A_wq, A_wo, B_x, B_w_gate, B_w_up, B_w_down, c, w_mod):

    def _A_ref(x, k_cache, v_cache, wq, wo):
        B, Sq, D = x.shape
        H, Dh = (16, 256)
        q = (lambda x, *d: x.permute(*d))((x @ wq).reshape(B, Sq, H, Dh), 0, 2, 1, 3)
        scores = torch.einsum('bhqd,bhkd->bhqk', q, k_cache) * Dh ** (-0.5)
        p = _softmax(scores, axis=-1)
        o = torch.einsum('bhqk,bhkd->bhqd', p, v_cache)
        return (lambda x, *d: x.permute(*d))(o, 0, 2, 1, 3).reshape(B, Sq, H * Dh) @ wo

    def _B_ref(x, w_gate, w_up, w_down):
        g = torch.einsum('bsd,df->bsf', x, w_gate)
        u = torch.einsum('bsd,df->bsf', x, w_up)
        h = _sigmoid(g) * u
        return torch.einsum('bsf,fd->bsd', h, w_down)
    x = A_x
    mod = (_silu(c) @ w_mod).reshape(c.shape[0], 6, 1, -1)
    sa, ca, ga, sb, cb, gb = (mod[:, 0].clone(), mod[:, 1].clone(), mod[:, 2].clone(), mod[:, 3].clone(), mod[:, 4].clone(), mod[:, 5].clone())
    h = _layernorm(x, axis=-1) * (1 + ca) + sa
    x = x + ga * _A_ref(h, A_k_cache, A_v_cache, A_wq, A_wo)
    h = _layernorm(x, axis=-1) * (1 + cb) + sb
    x = x + gb * _B_ref(h, B_w_gate, B_w_up, B_w_down)
    return x
```

```cpp
#include <hip/hip_runtime.h>
#include <hip/hip_bf16.h>
#include <stdint.h>

typedef unsigned short u16;
typedef unsigned u32x4 __attribute__((ext_vector_type(4)));
typedef __bf16 bf16x2 __attribute__((ext_vector_type(2)));

constexpr int D = 448, H = 16, SK = 8192, DH = 256, F = 1792, NB = 2;
constexpr int NSPLIT = 8;

__device__ __forceinline__ float bf2f(u16 v) { return __builtin_bit_cast(float, (unsigned)v << 16); }
__device__ __forceinline__ float rbf(float f) {
  unsigned u = __builtin_bit_cast(unsigned, f);
  u = (u + 0x7fffu + ((u >> 16) & 1u)) & 0xffff0000u;
  return __builtin_bit_cast(float, u);
}
__device__ __forceinline__ u16 f2bf(float f) { return (u16)(__builtin_bit_cast(unsigned, rbf(f)) >> 16); }
__device__ __forceinline__ float dot2(unsigned k, unsigned q, float acc) {
  return __builtin_amdgcn_fdot2_f32_bf16(__builtin_bit_cast(bf16x2, k), __builtin_bit_cast(bf16x2, q), acc, false);
}
__device__ __forceinline__ float wave_sum(float v) {
  for (int o = 32; o > 0; o >>= 1) v += __shfl_xor(v, o);
  return v;
}
__device__ __forceinline__ float wave_max(float v) {
  for (int o = 32; o > 0; o >>= 1) v = fmaxf(v, __shfl_xor(v, o));
  return v;
}

__global__ void __launch_bounds__(256) k_mod(const u16* c, const u16* wmod, float* mod) {
  __shared__ float sc[2][D];
  __shared__ float red[4][2][64];
  int tid = threadIdx.x;
  for (int i = tid; i < 2 * D; i += 256) { float v = bf2f(c[i]); sc[i / D][i % D] = rbf(v / (1.f + __expf(-v))); }
  __syncthreads();
  int col = blockIdx.x * 64 + (tid & 63), g = tid >> 6;
  float a0 = 0.f, a1 = 0.f;
  for (int k = g * 112; k < g * 112 + 112; ++k) { float w = bf2f(wmod[(size_t)k * 2688 + col]); a0 += sc[0][k] * w; a1 += sc[1][k] * w; }
  red[g][0][tid & 63] = a0; red[g][1][tid & 63] = a1;
  __syncthreads();
  if (tid < 128) { int b = tid >> 6, l = tid & 63; float s = red[0][b][l] + red[1][b][l] + red[2][b][l] + red[3][b][l];
    mod[b * 2688 + blockIdx.x * 64 + l] = rbf(s); }
}

__device__ __forceinline__ void ln_mod_row(const float* xrow, const float* shift, const float* scale, float* hrow, int lane) {
  float s = 0.f;
  for (int i = lane; i < D; i += 64) s += xrow[i];
  s = wave_sum(s);
  float mean = s * (1.f / D);
  float v = 0.f;
  for (int i = lane; i < D; i += 64) { float d = xrow[i] - mean; v += d * d; }
  v = wave_sum(v) * (1.f / D);
  float mb = rbf(mean), vb = rbf(v);
  float sd = rbf(sqrtf(rbf(vb + 1e-5f)));
  for (int i = lane; i < D; i += 64) {
    float ln = rbf(rbf(xrow[i] - mb) / sd);
    hrow[i] = rbf(rbf(ln * rbf(1.f + scale[i])) + shift[i]);
  }
}

__global__ void __launch_bounds__(256) k_q(const u16* x, const u16* wq, const float* mod, u16* q) {
  __shared__ float xs[2][D], hs[2][D];
  __shared__ float red[4][2][64];
  int tid = threadIdx.x;
  for (int i = tid; i < 2 * D; i += 256) xs[i / D][i % D] = bf2f(x[i]);
  __syncthreads();
  if (tid < 128) { int b = tid >> 6; ln_mod_row(xs[b], mod + b * 2688, mod + b * 2688 + D, hs[b], tid & 63); }
  __syncthreads();
  int col = blockIdx.x * 64 + (tid & 63), g = tid >> 6;
  float a0 = 0.f, a1 = 0.f;
  for (int k = g * 112; k < g * 112 + 112; ++k) { float w = bf2f(wq[(size_t)k * 4096 + col]); a0 += hs[0][k] * w; a1 += hs[1][k] * w; }
  red[g][0][tid & 63] = a0; red[g][1][tid & 63] = a1;
  __syncthreads();
  if (tid < 128) { int b = tid >> 6, l = tid & 63; float s = red[0][b][l] + red[1][b][l] + red[2][b][l] + red[3][b][l];
    q[b * 4096 + blockIdx.x * 64 + l] = f2bf(s); }
}

__global__ void __launch_bounds__(512) k_attn(const u16* __restrict__ K, const u16* __restrict__ V, const u16* __restrict__ q, float* part) {
  __shared__ float sm[8][DH + 2];
  int tid = threadIdx.x, lane = tid & 63, wave = tid >> 6;
  int bh = blockIdx.x >> 3, s = blockIdx.x & 7;
  int r = lane >> 5, c = lane & 31;
  u32x4 qv = *(const u32x4*)(q + bh * DH + 8 * c);
  const u16* kb = K + ((size_t)bh * SK + s * 1024 + wave * 128 + r) * DH + 8 * c;
  const u16* vb = V + ((size_t)bh * SK + s * 1024 + wave * 128 + r) * DH + 8 * c;
  float m = -INFINITY, l = 0.f, acc[8];
  for (int i = 0; i < 8; ++i) acc[i] = 0.f;
  constexpr int T = 8;
  for (int t = 0; t < 128 / (2 * T); ++t) {
    u32x4 kk[T], vv[T];
#pragma unroll
    for (int j = 0; j < T; ++j) kk[j] = __builtin_nontemporal_load((const u32x4*)(kb + (size_t)(t * 2 * T + 2 * j) * DH));
#pragma unroll
    for (int j = 0; j < T; ++j) vv[j] = __builtin_nontemporal_load((const u32x4*)(vb + (size_t)(t * 2 * T + 2 * j) * DH));
    float sc[T];
    float tm = -INFINITY;
#pragma unroll
    for (int j = 0; j < T; ++j) {
      float d = dot2(kk[j][0], qv[0], 0.f); d = dot2(kk[j][1], qv[1], d); d = dot2(kk[j][2], qv[2], d); d = dot2(kk[j][3], qv[3], d);
      for (int o = 16; o > 0; o >>= 1) d += __shfl_xor(d, o);
      sc[j] = rbf(d) * 0.0625f;
      tm = fmaxf(tm, sc[j]);
    }
    tm = fmaxf(tm, __shfl_xor(tm, 32));
    float mn = fmaxf(m, tm);
    float al = __expf(m - mn);
    l *= al;
#pragma unroll
    for (int i = 0; i < 8; ++i) acc[i] *= al;
#pragma unroll
    for (int j = 0; j < T; ++j) {
      float p = __expf(sc[j] - mn);
      l += p;
#pragma unroll
      for (int i = 0; i < 4; ++i) {
        acc[2 * i] += p * __builtin_bit_cast(float, vv[j][i] << 16);
        acc[2 * i + 1] += p * __builtin_bit_cast(float, vv[j][i] & 0xffff0000u);
      }
    }
    m = mn;
  }
  l += __shfl_xor(l, 32);
#pragma unroll
  for (int i = 0; i < 8; ++i) acc[i] += __shfl_xor(acc[i], 32);
  if (r == 0) {
#pragma unroll
    for (int i = 0; i < 8; ++i) sm[wave][8 * c + i] = acc[i];
    if (c == 0) { sm[wave][DH] = m; sm[wave][DH + 1] = l; }
  }
  __syncthreads();
  if (tid < DH) {
    float M = -INFINITY;
    for (int w = 0; w < 8; ++w) M = fmaxf(M, sm[w][DH]);
    float L = 0.f, o = 0.f;
    for (int w = 0; w < 8; ++w) { float e = __expf(sm[w][DH] - M); L += e * sm[w][DH + 1]; o += e * sm[w][tid]; }
    float* p = part + (size_t)blockIdx.x * (DH + 2);
    p[tid] = o;
    if (tid == 0) { p[DH] = M; p[DH + 1] = L; }
  }
}

__global__ void __launch_bounds__(448) k_wo(const float* part, const u16* wo, float* ypart) {
  __shared__ float os[2][64];
  int tid = threadIdx.x;
  int h = blockIdx.x >> 2, d0 = (blockIdx.x & 3) * 64;
  if (tid < 128) {
    int b = tid >> 6, d = d0 + (tid & 63);
    const float* p = part + (size_t)((b * H + h) * NSPLIT) * (DH + 2);
    float M = -INFINITY;
    for (int s = 0; s < NSPLIT; ++s) M = fmaxf(M, p[s * (DH + 2) + DH]);
    float L = 0.f, o = 0.f;
    for (int s = 0; s < NSPLIT; ++s) { float e = __expf(p[s * (DH + 2) + DH] - M); L += e * p[s * (DH + 2) + DH + 1]; o += e * p[s * (DH + 2) + d]; }
    os[b][tid & 63] = rbf(o / L);
  }
  __syncthreads();
  float a0 = 0.f, a1 = 0.f;
  for (int k = 0; k < 64; ++k) { float w = bf2f(wo[(size_t)(blockIdx.x * 64 + k) * D + tid]); a0 += os[0][k] * w; a1 += os[1][k] * w; }
  ypart[(size_t)blockIdx.x * 896 + tid] = a0;
  ypart[(size_t)blockIdx.x * 896 + D + tid] = a1;
}

__global__ void __launch_bounds__(448) k_ffn(const u16* x, const float* mod, const float* ypart, const u16* wg, const u16* wu, const u16* wd,
                                             float* x1out, float* opart) {
  __shared__ float xs[2][D], hs[2][D];
  __shared__ float red[14][4][32];
  __shared__ float hh[2][32];
  int tid = threadIdx.x;
  for (int b = 0; b < 2; ++b) {
    float s = 0.f;
    for (int p = 0; p < 64; ++p) s += ypart[(size_t)p * 896 + b * D + tid];
    float v = rbf(bf2f(x[b * D + tid]) + rbf(mod[b * 2688 + 2 * D + tid] * rbf(s)));
    xs[b][tid] = v;
    if (blockIdx.x == 0) x1out[b * D + tid] = v;
  }
  __syncthreads();
  if (tid < 128) { int b = tid >> 6; ln_mod_row(xs[b], mod + b * 2688 + 3 * D, mod + b * 2688 + 4 * D, hs[b], tid & 63); }
  __syncthreads();
  {
    int f = blockIdx.x * 32 + (tid & 31), g = tid >> 5;
    float g0 = 0.f, g1 = 0.f, u0 = 0.f, u1 = 0.f;
    for (int k = g * 32; k < g * 32 + 32; ++k) {
      float a = bf2f(wg[(size_t)k * F + f]), bb = bf2f(wu[(size_t)k * F + f]);
      g0 += hs[0][k] * a; g1 += hs[1][k] * a; u0 += hs[0][k] * bb; u1 += hs[1][k] * bb;
    }
    red[g][0][tid & 31] = g0; red[g][1][tid & 31] = g1; red[g][2][tid & 31] = u0; red[g][3][tid & 31] = u1;
  }
  __syncthreads();
  if (tid < 64) {
    int b = tid >> 5, f = tid & 31;
    float g = 0.f, u = 0.f;
    for (int i = 0; i < 14; ++i) { g += red[i][b][f]; u += red[i][2 + b][f]; }
    g = rbf(g); u = rbf(u);
    float sg = rbf(1.f / (1.f + __expf(-g)));
    hh[b][f] = rbf(sg * u);
  }
  __syncthreads();
  float a0 = 0.f, a1 = 0.f;
  for (int k = 0; k < 32; ++k) { float w = bf2f(wd[(size_t)(blockIdx.x * 32 + k) * D + tid]); a0 += hh[0][k] * w; a1 += hh[1][k] * w; }
  opart[(size_t)blockIdx.x * 896 + tid] = a0;
  opart[(size_t)blockIdx.x * 896 + D + tid] = a1;
}

__global__ void __launch_bounds__(64) k_final(const float* mod, const float* x1, const float* opart, u16* out) {
  int i = blockIdx.x * 64 + threadIdx.x;
  int b = i / D, d = i % D;
  float s = 0.f;
  for (int p = 0; p < 56; ++p) s += opart[(size_t)p * 896 + i];
  out[i] = f2bf(x1[i] + rbf(mod[b * 2688 + 5 * D + d] * rbf(s)));
}

extern "C" void kernel_launch(void* const* d_in, const int* in_sizes, int n_in, void* d_out, int out_size, void* d_ws, size_t ws_size,
                              hipStream_t stream) {
  const u16* x = (const u16*)d_in[0];
  const u16* K = (const u16*)d_in[1];
  const u16* V = (const u16*)d_in[2];
  const u16* wq = (const u16*)d_in[3];
  const u16* wo = (const u16*)d_in[4];
  const u16* wg = (const u16*)d_in[6];
  const u16* wu = (const u16*)d_in[7];
  const u16* wd = (const u16*)d_in[8];
  const u16* c = (const u16*)d_in[9];
  const u16* wmod = (const u16*)d_in[10];
  char* ws = (char*)d_ws;
  float* mod = (float*)ws;
  u16* q = (u16*)(ws + 32768);
  float* part = (float*)(ws + 65536);
  float* ypart = (float*)(ws + 524288);
  float* x1 = (float*)(ws + 1048576);
  float* opart = (float*)(ws + 1048576 + 65536);
  k_mod<<<42, 256, 0, stream>>>(c, wmod, mod);
  k_q<<<64, 256, 0, stream>>>(x, wq, mod, q);
  k_attn<<<256, 512, 0, stream>>>(K, V, q, part);
  k_wo<<<64, 448, 0, stream>>>(part, wo, ypart);
  k_ffn<<<56, 448, 0, stream>>>(x, mod, ypart, wg, wu, wd, x1, opart);
  k_final<<<14, 64, 0, stream>>>(mod, x1, opart, (u16*)d_out);
}
```

```cpp
#include <hip/hip_runtime.h>
#include <stdint.h>

typedef unsigned short u16;
typedef unsigned long long u64;
typedef unsigned u32x4 __attribute__((ext_vector_type(4)));
typedef unsigned u32x2 __attribute__((ext_vector_type(2)));
typedef float f32x4 __attribute__((ext_vector_type(4)));
typedef float f32x2 __attribute__((ext_vector_type(2)));
typedef __bf16 bf16x2 __attribute__((ext_vector_type(2)));
typedef __bf16 bf16x8 __attribute__((ext_vector_type(8)));
typedef __amdgpu_buffer_rsrc_t rsrc_t;
#define LAS __attribute__((address_space(3)))

constexpr int D = 448, FF = 1792;
constexpr int AUX_NT = 2, AUX_SC1 = 16;
constexpr int OOB = 0x40000000;
constexpr unsigned SPIN_CAP = 1u << 18;
constexpr float SC2 = 0.0625f * 1.4426950408889634f;

constexpr int W_E = 0;
constexpr int W_H = 4096;
constexpr int W_X = 8192;
constexpr int W_M2 = 12288;
constexpr int W_M3 = 20480;
constexpr int W_ML = 28672;
constexpr int W_Q = 32768;
constexpr int W_A = 65536;
constexpr int W_B = W_A + 256 * 2048;
constexpr int W_D = W_B + 2 * 64 * 3584;
constexpr int W_END = W_D + 2 * 56 * 3584;

#define STAMP(i) do {} while (0)

__device__ __forceinline__ float asf(unsigned u) { return __builtin_bit_cast(float, u); }
__device__ __forceinline__ unsigned asu(float f) { return __builtin_bit_cast(unsigned, f); }
__device__ __forceinline__ float blo(unsigned u) { return asf(u << 16); }
__device__ __forceinline__ float bhi(unsigned u) { return asf(u & 0xffff0000u); }
__device__ __forceinline__ unsigned pk(float lo, float hi) {
  bf16x2 v = {(__bf16)lo, (__bf16)hi};
  return __builtin_bit_cast(unsigned, v);
}
__device__ __forceinline__ float rbf(float f) { return blo(pk(f, f)); }
__device__ __forceinline__ float dot2(unsigned a, unsigned b, float c) {
  return __builtin_amdgcn_fdot2_f32_bf16(__builtin_bit_cast(bf16x2, a), __builtin_bit_cast(bf16x2, b), c, false);
}
template <int CTRL> __device__ __forceinline__ float dpp(float x) {
  return asf((unsigned)__builtin_amdgcn_mov_dpp((int)asu(x), CTRL, 0xf, 0xf, true));
}
constexpr int X1 = 0xB1, X2 = 0x4E, X7 = 0x141, X8 = 0x128;
__device__ __forceinline__ float tr16(float a, float b) {
  auto s = __builtin_amdgcn_permlane16_swap(asu(a), asu(b), false, false);
  return asf(s[0]) + asf(s[1]);
}
__device__ __forceinline__ float tr32(float a, float b) {
  auto s = __builtin_amdgcn_permlane32_swap(asu(a), asu(b), false, false);
  return asf(s[0]) + asf(s[1]);
}
__device__ __forceinline__ float mx16(float a) { auto s = __builtin_amdgcn_permlane16_swap(asu(a), asu(a), false, false); return fmaxf(asf(s[0]), asf(s[1])); }
__device__ __forceinline__ float mx32(float a) { auto s = __builtin_amdgcn_permlane32_swap(asu(a), asu(a), false, false); return fmaxf(asf(s[0]), asf(s[1])); }
__device__ __forceinline__ float row_sum(float v) { v += dpp<X8>(v); v += dpp<X7>(v); v += dpp<X2>(v); v += dpp<X1>(v); return v; }
__device__ __forceinline__ float half_sum(float v) { v = row_sum(v); return tr16(v, v); }
__device__ __forceinline__ float wave_sum(float v) { v = half_sum(v); return tr32(v, v); }
__device__ __forceinline__ rsrc_t mkr(const void* p, unsigned bytes) { return __builtin_amdgcn_make_buffer_rsrc((void*)p, 0, bytes, 0x00020000); }
__device__ __forceinline__ u32x4 ld_nt(rsrc_t r, int vo, int so) { return __builtin_amdgcn_raw_buffer_load_b128(r, vo, so, AUX_NT); }
__device__ __forceinline__ u32x4 ld_dev(rsrc_t r, int vo) { return __builtin_amdgcn_raw_buffer_load_b128(r, vo, 0, AUX_SC1); }
__device__ __forceinline__ void st_dev16(rsrc_t r, int vo, unsigned a, unsigned b, unsigned tag) {
  u32x4 v = {a, tag, b, tag};
  __builtin_amdgcn_raw_buffer_store_b128(v, r, vo, 0, AUX_SC1);
}
__device__ __forceinline__ void st_dev8(rsrc_t r, int vo, unsigned a, unsigned tag) {
  u32x2 v = {a, tag};
  __builtin_amdgcn_raw_buffer_store_b64(v, r, vo, 0, AUX_SC1);
}
#define SB() __builtin_amdgcn_sched_barrier(0)
#define BAR() do { asm volatile("s_waitcnt lgkmcnt(0)" ::: "memory"); __builtin_amdgcn_s_barrier(); asm volatile("" ::: "memory"); } while (0)

template <int N>
__device__ __forceinline__ void sweep(rsrc_t r, const int (&vo)[N], bool live, unsigned tag, u32x4 (&g)[N]) {
  #pragma nounroll
  for (unsigned spin = 0; spin < SPIN_CAP; ++spin) {
    asm volatile("" ::: "memory");
    bool ok = true;
#pragma unroll
    for (int i = 0; i < N; ++i) g[i] = ld_dev(r, vo[i]);
#pragma unroll
    for (int i = 0; i < N; ++i) ok = ok && g[i][1] == tag && g[i][3] == tag;
    if (__all(ok || !live)) break;
    __builtin_amdgcn_s_sleep(1);
  }
  asm volatile("" ::: "memory");
}

__device__ __forceinline__ void transpose8(const u32x4 (&w)[8], u32x4 (&f)[8]) {
#pragma unroll
  for (int d = 0; d < 4; ++d)
#pragma unroll
    for (int e = 0; e < 4; ++e) {
      f[2 * d][e] = __builtin_amdgcn_perm(w[2 * e + 1][d], w[2 * e][d], 0x05040100u);
      f[2 * d + 1][e] = __builtin_amdgcn_perm(w[2 * e + 1][d], w[2 * e][d], 0x07060302u);
    }
}
__device__ __forceinline__ f32x4 mfma(u32x4 a, u32x4 b, f32x4 c) {
  return __builtin_amdgcn_mfma_f32_16x16x32_bf16(__builtin_bit_cast(bf16x8, a), __builtin_bit_cast(bf16x8, b), c, 0, 0, 0);
}

__device__ __forceinline__ void dual_load(rsrc_t r1, rsrc_t r2, bool same, int c1, int c2, int ldb, int sw, int lane, u32x4 (&f)[2][8]) {
  const int row0 = 32 * ((lane >> 3) & 1) + 8 * (lane >> 4);
  const bool second = (lane & 4) != 0;
  const int vo = row0 * ldb + (second ? c2 : c1) + (lane & 3) * 16;
  u32x4 w[2][8];
#pragma unroll
  for (int u = 0; u < 2; ++u)
#pragma unroll
    for (int j = 0; j < 8; ++j) {
      const int so = (64 * (2 * sw + u) + j) * ldb;
      const bool dead = (u == 1) && (sw == 3);
      if (same) w[u][j] = ld_nt(r1, dead ? OOB : vo, dead ? 0 : so);
      else {
        const u32x4 a = ld_nt(r1, (dead || second) ? OOB : vo, dead ? 0 : so), b = ld_nt(r2, (dead || !second) ? OOB : vo, dead ? 0 : so);
        w[u][j] = a | b;
      }
    }
  transpose8(w[0], f[0]);
  transpose8(w[1], f[1]);
}
__device__ __forceinline__ void dual_mma(const u32x4 (&a)[2], const u32x4 (&f)[2][8], LAS float* P, int sw, int lane) {
  f32x4 acc[8];
#pragma unroll
  for (int i = 0; i < 8; ++i) acc[i] = mfma(a[0], f[0][i], f32x4{0.f, 0.f, 0.f, 0.f});
#pragma unroll
  for (int i = 0; i < 8; ++i) acc[i] = mfma(a[1], f[1][i], acc[i]);
  if (((lane >> 3) & 1) == (lane >> 5)) {
    LAS f32x4* p = (LAS f32x4*)(P + (sw * 4 + (lane >> 5) * 2 + ((lane >> 4) & 1)) * 64 + 8 * (lane & 7));
    p[0] = f32x4{acc[0][0], acc[1][0], acc[2][0], acc[3][0]};
    p[1] = f32x4{acc[4][0], acc[5][0], acc[6][0], acc[7][0]};
  }
}
__device__ __forceinline__ void dual_fin(const LAS float* P, int lane, float& v1, float& v2) {
  const LAS float* p = P + (lane >> 5) * 64 + (lane & 31);
  v1 = 0.f; v2 = 0.f;
#pragma unroll
  for (int i = 0; i < 8; ++i) { v1 += p[i * 128]; v2 += p[i * 128 + 32]; }
}
__device__ __forceinline__ void lds_arrive(LAS unsigned* c, int lane) {
  asm volatile("s_waitcnt lgkmcnt(0)" ::: "memory");
  if (lane == 0) __hip_atomic_fetch_add(c, 1u, __ATOMIC_RELAXED, __HIP_MEMORY_SCOPE_WORKGROUP);
}
__device__ __forceinline__ void lds_wait(LAS unsigned* c, unsigned n) {
  for (unsigned spin = 0; spin < (SPIN_CAP << 4); ++spin)
    if (__hip_atomic_load(c, __ATOMIC_RELAXED, __HIP_MEMORY_SCOPE_WORKGROUP) >= n) break;
  asm volatile("" ::: "memory");
}

template <int NS>
__device__ __forceinline__ void wide_load(rsrc_t r, int row0, int sw, int lane, u32x4 (&f)[NS][8]) {
  const int chunk = 16 * sw + (lane & 15);
  const int vo = chunk < 56 ? 8 * (lane >> 4) * 896 + chunk * 16 : OOB;
  u32x4 w[NS][8];
#pragma unroll
  for (int u = 0; u < NS; ++u)
#pragma unroll
    for (int j = 0; j < 8; ++j) w[u][j] = ld_nt(r, vo, (row0 + 32 * u + j) * 896);
#pragma unroll
  for (int u = 0; u < NS; ++u) transpose8(w[u], f[u]);
}
template <int NS>
__device__ __forceinline__ void wide_mma_publish(const u32x4 (&a)[NS], const u32x4 (&f)[NS][8], rsrc_t rw, int base, int np, int p, unsigned tag, int sw, int lane) {
  f32x4 acc[8];
#pragma unroll
  for (int i = 0; i < 8; ++i) acc[i] = mfma(a[0], f[0][i], f32x4{0.f, 0.f, 0.f, 0.f});
#pragma unroll
  for (int u = 1; u < NS; ++u)
#pragma unroll
    for (int i = 0; i < 8; ++i) acc[i] = mfma(a[u], f[u][i], acc[i]);
  const int chunk = 16 * sw + (lane & 15), g = lane >> 4;
  const bool up = g >= 2;
  const int vo = chunk < 56 ? base + ((g & 1) * np + p) * 3584 + chunk * 64 + (g >> 1) * 32 : OOB;
  st_dev16(rw, vo, asu(up ? acc[4][0] : acc[0][0]), asu(up ? acc[5][0] : acc[1][0]), tag);
  st_dev16(rw, chunk < 56 ? vo + 16 : OOB, asu(up ? acc[6][0] : acc[2][0]), asu(up ? acc[7][0] : acc[3][0]), tag);
}
__device__ __forceinline__ void reduce8(rsrc_t rw, int base, int np, int b, int chunk, unsigned tag, int lane, float& s0, float& s1) {
  const bool live = lane < np;
  const int o = live ? base + (b * np + lane) * 3584 + chunk * 64 : OOB;
  const int vo[4] = {o, live ? o + 16 : OOB, live ? o + 32 : OOB, live ? o + 48 : OOB};
  u32x4 g[4];
  sweep<4>(rw, vo, live, tag, g);
  const float w0 = tr32(asf(g[0][0]), asf(g[2][0])), w1 = tr32(asf(g[0][2]), asf(g[2][2]));
  const float w2 = tr32(asf(g[1][0]), asf(g[3][0])), w3 = tr32(asf(g[1][2]), asf(g[3][2]));
  s0 = row_sum(tr16(w0, w2));
  s1 = row_sum(tr16(w1, w3));
}

__device__ __forceinline__ float tile_qk(const u32x4 (&k)[8], const u32x4 q, float& m, float& l, float (&acc)[8], int lane) {
  float d[8];
#pragma unroll
  for (int j = 0; j < 8; ++j) d[j] = dot2(k[j][3], q[3], dot2(k[j][2], q[2], dot2(k[j][1], q[1], dot2(k[j][0], q[0], 0.f))));
  const bool b8 = lane & 8, b4 = lane & 4, b2 = lane & 2;
#pragma unroll
  for (int j = 0; j < 4; ++j) d[j] = (b8 ? d[j + 4] : d[j]) + dpp<X8>(b8 ? d[j] : d[j + 4]);
#pragma unroll
  for (int j = 0; j < 2; ++j) d[j] = (b4 ? d[j + 2] : d[j]) + dpp<X7>(b4 ? d[j] : d[j + 2]);
  float s = (b2 ? d[1] : d[0]) + dpp<X2>(b2 ? d[0] : d[1]);
  s += dpp<X1>(s);
  s = rbf(tr16(s, s));
  if (__any(s > m)) {
    float t = fmaxf(s, dpp<X2>(s)); t = fmaxf(t, dpp<X7>(t)); t = fmaxf(t, dpp<X8>(t)); t = mx32(t);
    const float al = __builtin_amdgcn_exp2f((m - t) * SC2);
    l *= al;
#pragma unroll
    for (int i = 0; i < 8; ++i) acc[i] *= al;
    m = t;
  }
  const float p = __builtin_amdgcn_exp2f((s - m) * SC2);
  l += p;
  return p;
}
template <int J> __device__ __forceinline__ void pv_row(const u32x4 v, float p, float (&acc)[8]) {
  const float pj = dpp<0x150 + 2 * J>(p);
#pragma unroll
  for (int i = 0; i < 4; ++i) { acc[2 * i] = fmaf(pj, blo(v[i]), acc[2 * i]); acc[2 * i + 1] = fmaf(pj, bhi(v[i]), acc[2 * i + 1]); }
}
__device__ __forceinline__ void tile_pv(const u32x4 (&v)[8], float p, float (&acc)[8]) {
  pv_row<0>(v[0], p, acc); pv_row<1>(v[1], p, acc); pv_row<2>(v[2], p, acc); pv_row<3>(v[3], p, acc);
  pv_row<4>(v[4], p, acc); pv_row<5>(v[5], p, acc); pv_row<6>(v[6], p, acc); pv_row<7>(v[7], p, acc);
}
#define PIN8(a) asm volatile("" : "+v"(a[0]), "+v"(a[1]), "+v"(a[2]), "+v"(a[3]), "+v"(a[4]), "+v"(a[5]), "+v"(a[6]), "+v"(a[7]))

constexpr int L_CNT = 0, L_TAG = 16, L_Q = 64, L_P = 1024, L_A = 5120, L_O = 7168, L_ML = 11264, L_HH = 11520, L_END = 12288;

__global__ void __launch_bounds__(512, 2)
k_block(const u16* __restrict__ x, const u16* __restrict__ Kc, const u16* __restrict__ Vc, const u16* __restrict__ wq, const u16* __restrict__ wo,
        const u16* __restrict__ wg, const u16* __restrict__ wu, const u16* __restrict__ wd, const u16* __restrict__ cv,
        const u16* __restrict__ wm, u16* __restrict__ out, unsigned char* ws) {
  __shared__ __attribute__((aligned(16))) unsigned char smem[L_END];
  LAS unsigned char* L = (LAS unsigned char*)smem;
  const int tid = threadIdx.x, lane = tid & 63, wave = __builtin_amdgcn_readfirstlane(tid >> 6);
  const int bx = blockIdx.x & 7, br = blockIdx.x >> 3;
  const int pair = br, split = (bx + br) & 7;
  if (tid == 0) *(LAS unsigned*)(L + L_CNT) = 0u;
  asm volatile("" :: "s"(x), "s"(Kc), "s"(Vc), "s"(wq), "s"(wo), "s"(wg), "s"(wu), "s"(wd), "s"(cv), "s"(wm), "s"(out), "s"(ws));
  BAR();
  const rsrc_t rw = mkr(ws, W_END);

  if (wave < 4) {
    STAMP(0);
    const rsrc_t rk = mkr(Kc + ((size_t)pair * 8192 + split * 1024) * 256, 524288);
    const rsrc_t rv = mkr(Vc + ((size_t)pair * 8192 + split * 1024) * 256, 524288);
    const int vo = lane * 16 + wave * 1024;
    u32x4 kS[8], vS[8], kA[8], vA[8], kB[8], vB[8];
#define ISSUE(buf, r, t) _Pragma("unroll") for (int j = 0; j < 8; ++j) { buf[j] = ld_nt(r, vo, (t) * 32768 + j * 4096); SB(); }
#define ISSUE_PACED(buf, r, t) _Pragma("unroll") for (int j = 0; j < 8; ++j) { asm volatile("s_waitcnt vmcnt(7)" ::: "memory"); buf[j] = ld_nt(r, vo, (t) * 32768 + j * 4096); SB(); }
    ISSUE(kS, rk, 0)
    ISSUE_PACED(vS, rv, 0) ISSUE_PACED(kA, rk, 1) ISSUE_PACED(vA, rv, 1) ISSUE_PACED(kB, rk, 2) ISSUE_PACED(vB, rv, 2)
    STAMP(1);
    BAR();
    STAMP(2);
    const u32x4 q = *(const LAS u32x4*)(L + L_Q + (lane & 31) * 16);
    float m = -INFINITY, l = 0.f, acc[8];
#pragma unroll
    for (int i = 0; i < 8; ++i) acc[i] = 0.f;
    float p;
    p = tile_qk(kS, q, m, l, acc, lane); asm volatile("" : "+v"(p)); SB();
    ISSUE(kS, rk, 3)
    tile_pv(vS, p, acc); PIN8(acc); SB();
    ISSUE(vS, rv, 3)
    p = tile_qk(kA, q, m, l, acc, lane); tile_pv(vA, p, acc); PIN8(acc); SB();
    p = tile_qk(kB, q, m, l, acc, lane); tile_pv(vB, p, acc); PIN8(acc); SB();
    STAMP(3);
    for (int t = 4; t < 16; ++t) {
      p = tile_qk(kS, q, m, l, acc, lane); asm volatile("" : "+v"(p)); SB();
      ISSUE(kS, rk, t)
      tile_pv(vS, p, acc); PIN8(acc); SB();
      ISSUE(vS, rv, t)
    }
    p = tile_qk(kS, q, m, l, acc, lane); tile_pv(vS, p, acc);
    l = wave_sum(l) * 0.25f;
    {
      f32x4 o = {tr32(acc[0], acc[4]), tr32(acc[1], acc[5]), tr32(acc[2], acc[6]), tr32(acc[3], acc[7])};
      *(LAS f32x4*)(L + L_O + wave * 1024 + (lane & 31) * 32 + (lane >> 5) * 16) = o;
      if (lane == 0) *(LAS f32x2*)(L + L_ML + wave * 8) = f32x2{m, l};
    }
    STAMP(4);
    BAR();
    if (wave < 2) {
      const f32x4 ml0 = *(const LAS f32x4*)(L + L_ML), ml1 = *(const LAS f32x4*)(L + L_ML + 16);
      const float M = fmaxf(fmaxf(ml0[0], ml0[2]), fmaxf(ml1[0], ml1[2]));
      const float w0 = __builtin_amdgcn_exp2f((ml0[0] - M) * SC2), w1 = __builtin_amdgcn_exp2f((ml0[2] - M) * SC2);
      const float w2 = __builtin_amdgcn_exp2f((ml1[0] - M) * SC2), w3 = __builtin_amdgcn_exp2f((ml1[2] - M) * SC2);
      const LAS f32x2* po = (const LAS f32x2*)(L + L_O) + tid;
      const f32x2 o = w0 * po[0] + w1 * po[128] + w2 * po[256] + w3 * po[384];
      const int slot = pair * 8 + split;
      const unsigned tag = *(const LAS unsigned*)(L + L_TAG);
      st_dev16(rw, W_A + slot * 2048 + tid * 16, asu(o[0]), asu(o[1]), tag);
      st_dev16(rw, tid == 0 ? W_ML + slot * 16 : OOB, asu(M), asu(w0 * ml0[1] + w1 * ml0[3] + w2 * ml1[1] + w3 * ml1[3]), tag);
      STAMP(5);
    }
    return;
  }

  const int sw = wave - 4;
  LAS float* P = (LAS float*)(L + L_P);
  LAS unsigned* cnt = (LAS unsigned*)(L + L_CNT);
  const int arow_b = (lane >> 2) & 1, arow_s = (lane >> 3) & 1, kg = lane >> 4;
  const bool roleM = bx < 7 && (br < 2 || (br >= 10 && br < 14));
  const bool roleQ = br >= 2 && br < 10;
  const bool roleS1 = br >= 16 && br < 24;
  const bool roleS3 = bx < 7 && br >= 24;
  const bool roleR = bx < 7 && br < 16 && sw == 0;
  unsigned ev = __builtin_amdgcn_raw_buffer_load_b32(rw, W_E, 0, 0);
  unsigned e0, tag;
#define GET_TAG() do { asm volatile("" : "+v"(ev)); e0 = __builtin_amdgcn_readfirstlane(ev); \
    tag = (e0 ^ (e0 >> 16)) * 0x7feb352du; tag = (tag ^ (tag >> 15)) * 0x846ca68bu; tag = (tag ^ (tag >> 16)) + 0x9e3779b9u; } while (0)

  if (roleM) {
    const int mi = br < 2 ? 0 : ((br - 8) >> 1), cg = bx * 2 + (br & 1);
    const int ch1 = mi == 0 ? 0 : (mi == 1 ? 3 : 2), ch2 = mi == 0 ? 1 : (mi == 1 ? 4 : 5);
    const rsrc_t rm = mkr(wm, D * 6 * D * 2), rc = mkr(cv, 2 * D * 2), rx = mkr(x, 2 * D * 2);
    u32x4 cr[2];
#pragma unroll
    for (int u = 0; u < 2; ++u) {
      const int k0 = 64 * (2 * sw + u) + 32 * arow_s + 8 * kg;
      cr[u] = __builtin_amdgcn_raw_buffer_load_b128(rc, k0 < D ? (arow_b * D + k0) * 2 : OOB, 0, 0);
    }
    float xv[14];
#pragma unroll
    for (int i = 0; i < 14; ++i) xv[i] = blo(__builtin_amdgcn_raw_buffer_load_b16(rx, ((lane >> 5) * D + 32 * i + (lane & 31)) * 2, 0, 0));
    const float xo = blo(__builtin_amdgcn_raw_buffer_load_b16(rx, ((lane >> 5) * D + 32 * cg + (lane & 31)) * 2, 0, 0));
    u32x4 f[2][8];
    dual_load(rm, rm, true, (ch1 * D + 32 * cg) * 2, (ch2 * D + 32 * cg) * 2, 6 * D * 2, sw, lane, f);
    GET_TAG();
    u32x4 a[2];
#pragma unroll
    for (int u = 0; u < 2; ++u)
#pragma unroll
      for (int i = 0; i < 4; ++i) {
        const float c0 = blo(cr[u][i]), c1 = bhi(cr[u][i]);
        a[u][i] = pk(c0 / (1.f + __expf(-c0)), c1 / (1.f + __expf(-c1)));
      }
    float s = 0.f;
#pragma unroll
    for (int i = 0; i < 14; ++i) s += xv[i];
    const float mean = half_sum(s) * (1.f / D);
    s = 0.f;
#pragma unroll
    for (int i = 0; i < 14; ++i) s += (xv[i] - mean) * (xv[i] - mean);
    const float sd = rbf(sqrtf(rbf(rbf(half_sum(s) * (1.f / D)) + 1e-5f)));
    const float ln = rbf(rbf(xo - rbf(mean)) / sd);
    dual_mma(a, f, P, sw, lane);
    lds_arrive(cnt, lane);
    if (sw == 0) {
      lds_wait(cnt, 4);
      float v1, v2;
      dual_fin(P, lane, v1, v2);
      v1 = rbf(v1); v2 = rbf(v2);
      const int col = (lane >> 5) * D + 32 * cg + (lane & 31);
      if (mi == 0) {
        const float h = rbf(ln * rbf(1.f + v2)) + v1;
        st_dev8(rw, (lane & 1) ? OOB : W_H + col * 4, pk(h, dpp<X1>(h)), tag);
      } else {
        st_dev8(rw, (mi == 1 ? W_M2 : W_M3) + col * 8, mi == 1 ? pk(1.f + v2, v1) : pk(v1, v2), tag);
      }
      STAMP(8);
    }
  } else if (roleQ) {
    const int cg = bx * 8 + br - 2;
    const rsrc_t rq = mkr(wq, D * 4096 * 2);
    u32x4 f[2][8];
    dual_load(rq, rq, true, cg * 128, cg * 128 + 64, 8192, sw, lane, f);
    GET_TAG();
    int vo[4];
#pragma unroll
    for (int u = 0; u < 2; ++u) {
      const int k0 = 64 * ((sw == 3) ? 6 : 2 * sw + u) + 32 * arow_s + 8 * kg;
      vo[2 * u] = W_H + (arow_b * D + k0) * 4; vo[2 * u + 1] = vo[2 * u] + 16;
    }
    u32x4 g[4];
    PIN8(f[0]); PIN8(f[1]);
    STAMP(9);
    sweep<4>(rw, vo, true, tag, g);
    STAMP(10);
    const u32x4 a[2] = {{g[0][0], g[0][2], g[1][0], g[1][2]}, {g[2][0], g[2][2], g[3][0], g[3][2]}};
    dual_mma(a, f, P, sw, lane);
    lds_arrive(cnt, lane);
    if (sw == 0) {
      lds_wait(cnt, 4);
      float v1, v2;
      dual_fin(P, lane, v1, v2);
      const float n1 = dpp<X1>(v1), n2 = dpp<X1>(v2);
      const bool odd = lane & 1;
      st_dev8(rw, W_Q + ((lane >> 5) * 4096 + 64 * cg + (odd ? 32 : 0) + (lane & 30)) * 4, odd ? pk(n2, v2) : pk(v1, n1), tag);
      STAMP(11);
    }
  } else {
    GET_TAG();
  }
  if (sw == 1) {
    const int vo[1] = {W_Q + pair * 1024 + lane * 16};
    u32x4 g[1];
    sweep<1>(rw, vo, true, tag, g);
    *(LAS u32x2*)(L + L_Q + lane * 8) = u32x2{g[0][0], g[0][2]};
    if (lane == 0) *(LAS unsigned*)(L + L_TAG) = tag;
    STAMP(12);
  }
  asm volatile("" :: "s"(tag));
  BAR();

  if (roleS1) {
    const int dg = bx * 8 + br - 16;
    u32x4 f[2][8];
    wide_load<2>(mkr(wo, 4096 * D * 2), dg * 64, sw, lane, f);
    PIN8(f[0]); PIN8(f[1]);
    BAR();
    const int b = sw >> 1, slot = (b * 16 + (dg >> 2)) * 8 + (lane >> 3);
    const int o = W_A + slot * 2048 + ((dg & 3) * 64 + (sw & 1) * 32 + (lane & 7) * 4) * 8;
    const int vo[3] = {o, o + 16, W_ML + slot * 16};
    u32x4 g[3];
    sweep<3>(rw, vo, true, tag, g);
    STAMP(16);
    const float ms = asf(g[2][0]);
    const float M = mx32(mx16(fmaxf(ms, dpp<X8>(ms))));
    const float w = __builtin_amdgcn_exp2f((ms - M) * SC2);
    float v[5] = {w * asf(g[0][0]), w * asf(g[0][2]), w * asf(g[1][0]), w * asf(g[1][2]), w * asf(g[2][2])};
#pragma unroll
    for (int i = 0; i < 5; ++i) { v[i] += dpp<X8>(v[i]); v[i] = tr16(v[i], v[i]); v[i] = tr32(v[i], v[i]); }
    const float inv = 1.f / v[4];
    if (lane < 8) *(LAS u32x2*)(L + L_A + b * 128 + (sw & 1) * 64 + lane * 8) = u32x2{pk(v[0] * inv, v[1] * inv), pk(v[2] * inv, v[3] * inv)};
    BAR();
    const u32x4 a[2] = {*(const LAS u32x4*)(L + L_A + arow_b * 128 + kg * 16), *(const LAS u32x4*)(L + L_A + arow_b * 128 + 64 + kg * 16)};
    wide_mma_publish<2>(a, f, rw, W_B, 64, dg, tag, sw, lane);
    STAMP(17);
  } else if (roleS3) {
    const int fg = bx * 8 + br - 24;
    u32x4 f[2][8], fd[1][8];
    dual_load(mkr(wg, D * FF * 2), mkr(wu, D * FF * 2), false, fg * 64, fg * 64, FF * 2, sw, lane, f);
    wide_load<1>(mkr(wd, FF * D * 2), fg * 32, sw, lane, fd);
    const bool live = lane < 56;
    u32x4 cs[4];
    if (sw < 2) {
      const int o = live ? W_M2 + (sw * D + lane * 8) * 8 : OOB;
      const int vo[4] = {o, live ? o + 16 : OOB, live ? o + 32 : OOB, live ? o + 48 : OOB};
      sweep<4>(rw, vo, live, tag, cs);
    }
    PIN8(f[0]); PIN8(f[1]); PIN8(fd[0]);
    BAR();
    if (sw < 2) {
      const int o = live ? W_X + (sw * D + lane * 8) * 4 : OOB;
      const int vo[2] = {o, live ? o + 16 : OOB};
      u32x4 g[2];
      sweep<2>(rw, vo, live, tag, g);
      STAMP(20);
      float xv[8] = {blo(g[0][0]), bhi(g[0][0]), blo(g[0][2]), bhi(g[0][2]), blo(g[1][0]), bhi(g[1][0]), blo(g[1][2]), bhi(g[1][2])};
      const float mean = wave_sum(((xv[0] + xv[1]) + (xv[2] + xv[3])) + ((xv[4] + xv[5]) + (xv[6] + xv[7]))) * (1.f / D);
      float s = 0.f;
#pragma unroll
      for (int i = 0; i < 8; ++i) s += (xv[i] - mean) * (xv[i] - mean);
      s = wave_sum(live ? s : 0.f);
      const float rs = 1.f / rbf(sqrtf(rbf(rbf(s * (1.f / D)) + 1e-5f)));
      const float mb = rbf(mean);
      u32x4 h;
#pragma unroll
      for (int i = 0; i < 4; ++i)
        h[i] = pk(fmaf((xv[2 * i] - mb) * rs, blo(cs[i][0]), bhi(cs[i][0])), fmaf((xv[2 * i + 1] - mb) * rs, blo(cs[i][2]), bhi(cs[i][2])));
      *(LAS u32x4*)(L + L_A + sw * 1024 + lane * 16) = h;
    }
    BAR();
    const LAS unsigned char* ap = L + L_A + arow_b * 1024 + (128 * sw + 32 * arow_s + 8 * kg) * 2;
    const u32x4 a[2] = {*(const LAS u32x4*)ap, *(const LAS u32x4*)(ap + 128)};
    dual_mma(a, f, P, sw, lane);
    BAR();
    float v1, v2;
    dual_fin(P, lane, v1, v2);
    const float hh = rbf(1.f / (1.f + __expf(-rbf(v1)))) * rbf(v2);
    *(LAS u16*)(L + L_HH + sw * 128 + lane * 2) = (u16)pk(hh, hh);
    asm volatile("s_waitcnt lgkmcnt(0)" ::: "memory");
    const u32x4 ad[1] = {*(const LAS u32x4*)(L + L_HH + sw * 128 + arow_b * 64 + kg * 16)};
    wide_mma_publish<1>(ad, fd, rw, W_D, 56, fg, tag, sw, lane);
    STAMP(21);
  } else if (roleR) {
    const int b = br >> 3, chunk = bx * 8 + (br & 7), col = b * D + chunk * 8 + 2 * kg;
    const unsigned xp = *(const unsigned*)(x + col);
    const int vo[1] = {W_M3 + col * 8};
    u32x4 gm[1];
    sweep<1>(rw, vo, true, tag, gm);
    BAR();
    float s0, s1;
    reduce8(rw, W_B, 64, b, chunk, tag, lane, s0, s1);
    STAMP(18);
    const unsigned x1 = pk(blo(xp) + rbf(blo(gm[0][0]) * rbf(s0)), bhi(xp) + rbf(blo(gm[0][2]) * rbf(s1)));
    st_dev8(rw, (lane & 15) ? OOB : W_X + col * 4, x1, tag);
    STAMP(19);
    reduce8(rw, W_D, 56, b, chunk, tag, lane, s0, s1);
    STAMP(22);
    if ((lane & 15) == 0) *(unsigned*)(out + col) = pk(blo(x1) + rbf(bhi(gm[0][0]) * rbf(s0)), bhi(x1) + rbf(bhi(gm[0][2]) * rbf(s1)));
    if (blockIdx.x == 0 && lane == 0) __builtin_amdgcn_raw_buffer_store_b32(e0 + 1u, rw, W_E, 0, AUX_SC1);
    STAMP(23);
  }
}

extern "C" void kernel_launch(void* const* d_in, const int* in_sizes, int n_in, void* d_out, int out_size, void* d_ws, size_t ws_size,
                              hipStream_t stream) {
  k_block<<<256, 512, 0, stream>>>((const u16*)d_in[0], (const u16*)d_in[1], (const u16*)d_in[2], (const u16*)d_in[3], (const u16*)d_in[4],
                                   (const u16*)d_in[6], (const u16*)d_in[7], (const u16*)d_in[8], (const u16*)d_in[9], (const u16*)d_in[10],
                                   (u16*)d_out, (unsigned char*)d_ws);
}
```

```cpp
#include <hip/hip_runtime.h>
#include <stdint.h>

typedef unsigned short u16;
typedef unsigned long long u64;
typedef unsigned u32x4 __attribute__((ext_vector_type(4)));
typedef unsigned u32x2 __attribute__((ext_vector_type(2)));
typedef float f32x4 __attribute__((ext_vector_type(4)));
typedef float f32x2 __attribute__((ext_vector_type(2)));
typedef __bf16 bf16x2 __attribute__((ext_vector_type(2)));
typedef __bf16 bf16x8 __attribute__((ext_vector_type(8)));
typedef __amdgpu_buffer_rsrc_t rsrc_t;
#define LAS __attribute__((address_space(3)))

#define TSLEEP 0
#define PIPE 1
#define PACE "6"
constexpr int D = 448, FF = 1792;
constexpr int AUX_NT = 2, AUX_SC1 = 16;
constexpr int OOB = 0x40000000;
constexpr unsigned SPIN_CAP = 1u << 18;
constexpr float SC2 = 0.0625f * 1.4426950408889634f;

constexpr int W_E = 0;
constexpr int MOD_REP = 2 * 1344 * 8, Q_REP = 2 * 2048 * 8, X_REP = 2 * 56 * 128;
constexpr int W_MOD = 4096;
constexpr int W_Q = W_MOD + 8 * MOD_REP;
constexpr int W_X = W_Q + 8 * Q_REP;
constexpr int W_A = W_X + 8 * X_REP;
constexpr int W_B = W_A + 256 * 2560;
constexpr int W_D = W_B + 64 * 7168;
constexpr int W_END = W_D + 56 * 7168;

#define STAMP(i) do {} while (0)
#define STAMP_INIT() do {} while (0)
#define STAMP_FLUSH() do {} while (0)

__device__ __forceinline__ float asf(unsigned u) { return __builtin_bit_cast(float, u); }
__device__ __forceinline__ unsigned asu(float f) { return __builtin_bit_cast(unsigned, f); }
__device__ __forceinline__ float blo(unsigned u) { return asf(u << 16); }
__device__ __forceinline__ float bhi(unsigned u) { return asf(u & 0xffff0000u); }
__device__ __forceinline__ unsigned pk(float lo, float hi) {
  bf16x2 v = {(__bf16)lo, (__bf16)hi};
  return __builtin_bit_cast(unsigned, v);
}
__device__ __forceinline__ float rbf(float f) { return blo(pk(f, f)); }
__device__ __forceinline__ float rcp(float f) { return __builtin_amdgcn_rcpf(f); }
__device__ __forceinline__ float ex2(float f) { return __builtin_amdgcn_exp2f(f); }
__device__ __forceinline__ float sigm(float f) { return rcp(1.f + ex2(f * -1.4426950408889634f)); }
__device__ __forceinline__ float dot2(unsigned a, unsigned b, float c) {
  return __builtin_amdgcn_fdot2_f32_bf16(__builtin_bit_cast(bf16x2, a), __builtin_bit_cast(bf16x2, b), c, false);
}
template <int CTRL> __device__ __forceinline__ unsigned dppu(unsigned x) { return (unsigned)__builtin_amdgcn_mov_dpp((int)x, CTRL, 0xf, 0xf, true); }
template <int CTRL> __device__ __forceinline__ float dpp(float x) { return asf(dppu<CTRL>(asu(x))); }
constexpr int X1 = 0xB1, X2 = 0x4E, X7 = 0x141, X8 = 0x128, R4 = 0x124;
__device__ __forceinline__ u32x4 quad_gather(unsigned v) { return u32x4{dppu<0x00>(v), dppu<0x55>(v), dppu<0xAA>(v), dppu<0xFF>(v)}; }
__device__ __forceinline__ float tr16(float a, float b) {
  auto s = __builtin_amdgcn_permlane16_swap(asu(a), asu(b), false, false);
  return asf(s[0]) + asf(s[1]);
}
__device__ __forceinline__ float tr32(float a, float b) {
  auto s = __builtin_amdgcn_permlane32_swap(asu(a), asu(b), false, false);
  return asf(s[0]) + asf(s[1]);
}
__device__ __forceinline__ float mx16(float a) { auto s = __builtin_amdgcn_permlane16_swap(asu(a), asu(a), false, false); return fmaxf(asf(s[0]), asf(s[1])); }
__device__ __forceinline__ float mx32(float a) { auto s = __builtin_amdgcn_permlane32_swap(asu(a), asu(a), false, false); return fmaxf(asf(s[0]), asf(s[1])); }
__device__ __forceinline__ float wave_sum(float v) { v += dpp<X8>(v); v += dpp<X7>(v); v += dpp<X2>(v); v += dpp<X1>(v); v = tr16(v, v); return tr32(v, v); }
__device__ __forceinline__ float batch_sum(float v) { v += dpp<X1>(v); v += dpp<X2>(v); v += dpp<X8>(v); v = tr16(v, v); return tr32(v, v); }
__device__ __forceinline__ rsrc_t mkr(const void* p, unsigned bytes) { return __builtin_amdgcn_make_buffer_rsrc((void*)p, 0, bytes, 0x00020000); }
__device__ __forceinline__ u32x4 ld_nt(rsrc_t r, int vo, int so) { return __builtin_amdgcn_raw_buffer_load_b128(r, vo, so, AUX_NT); }
__device__ __forceinline__ void st_dev16(rsrc_t r, int vo, unsigned a, unsigned b, unsigned tag) {
  __builtin_amdgcn_raw_buffer_store_b128(u32x4{a, tag, b, tag}, r, vo, 0, AUX_SC1);
}
#define SB() __builtin_amdgcn_sched_barrier(0)
#define BAR() do { asm volatile("s_waitcnt lgkmcnt(0)" ::: "memory"); __builtin_amdgcn_s_barrier(); asm volatile("" ::: "memory"); } while (0)
#define PIN8(a) asm volatile("" : "+v"(a[0]), "+v"(a[1]), "+v"(a[2]), "+v"(a[3]), "+v"(a[4]), "+v"(a[5]), "+v"(a[6]), "+v"(a[7]))

template <int N>
__device__ __forceinline__ void sweep16(rsrc_t r, const int (&vo)[N], unsigned tag, u32x4 (&g)[N]) {
#pragma nounroll
  for (unsigned spin = 0; spin < SPIN_CAP; ++spin) {
    asm volatile("" ::: "memory");
    bool ok = true;
#pragma unroll
    for (int i = 0; i < N; ++i) g[i] = __builtin_amdgcn_raw_buffer_load_b128(r, vo[i], 0, AUX_SC1);
#pragma unroll
    for (int i = 0; i < N; ++i) ok = ok & (((g[i][1] == tag) & (g[i][3] == tag)) | (vo[i] == OOB));
    if (__all(ok)) break;
    if (TSLEEP) __builtin_amdgcn_s_sleep(TSLEEP);
  }
  asm volatile("" ::: "memory");
}
template <int N, int SLEEP>
__device__ __forceinline__ void sweep8(rsrc_t r, const int (&vo)[N], unsigned tag, unsigned (&v)[N]) {
#pragma nounroll
  for (unsigned spin = 0; spin < SPIN_CAP; ++spin) {
    asm volatile("" ::: "memory");
    bool ok = true;
    u32x2 g[N];
#pragma unroll
    for (int i = 0; i < N; ++i) g[i] = __builtin_amdgcn_raw_buffer_load_b64(r, vo[i], 0, AUX_SC1);
#pragma unroll
    for (int i = 0; i < N; ++i) { ok = ok & ((g[i][1] == tag) | (vo[i] == OOB)); v[i] = g[i][0]; }
    if (__all(ok)) break;
    __builtin_amdgcn_s_sleep(SLEEP);
  }
  asm volatile("" ::: "memory");
}

__device__ __forceinline__ void transpose8(const u32x4 (&w)[8], u32x4 (&f)[8]) {
#pragma unroll
  for (int d = 0; d < 4; ++d)
#pragma unroll
    for (int e = 0; e < 4; ++e) {
      f[2 * d][e] = __builtin_amdgcn_perm(w[2 * e + 1][d], w[2 * e][d], 0x05040100u);
      f[2 * d + 1][e] = __builtin_amdgcn_perm(w[2 * e + 1][d], w[2 * e][d], 0x07060302u);
    }
}
__device__ __forceinline__ f32x4 mfma(u32x4 a, u32x4 b, f32x4 c) {
  return __builtin_amdgcn_mfma_f32_16x16x32_bf16(__builtin_bit_cast(bf16x8, a), __builtin_bit_cast(bf16x8, b), c, 0, 0, 0);
}

template <bool SUM2>
__device__ __forceinline__ void col_mma(const u32x4 (&a)[2], const u32x4 (&f)[2][8], LAS float* P, int sw, int lane) {
  f32x4 acc[8];
#pragma unroll
  for (int i = 0; i < 8; ++i) acc[i] = mfma(a[0], f[0][i], f32x4{0.f, 0.f, 0.f, 0.f});
#pragma unroll
  for (int i = 0; i < 8; ++i) acc[i] = mfma(a[1], f[1][i], acc[i]);
  float r[8];
#pragma unroll
  for (int i = 0; i < 8; ++i) r[i] = SUM2 ? acc[i][0] + acc[i][1] : acc[i][0];
  if (((lane >> 3) & 1) == (lane >> 5)) {
    LAS f32x4* p = (LAS f32x4*)(P + (sw * 4 + (lane >> 5) * 2 + ((lane >> 4) & 1)) * 64 + 8 * (lane & 7));
    p[0] = f32x4{r[0], r[1], r[2], r[3]};
    p[1] = f32x4{r[4], r[5], r[6], r[7]};
  }
}
__device__ __forceinline__ void lds_arrive(LAS unsigned* c, int lane) {
  asm volatile("s_waitcnt lgkmcnt(0)" ::: "memory");
  if (lane == 0) __hip_atomic_fetch_add(c, 1u, __ATOMIC_RELAXED, __HIP_MEMORY_SCOPE_WORKGROUP);
}
__device__ __forceinline__ void lds_wait(LAS unsigned* c, unsigned n) {
#pragma nounroll
  for (unsigned spin = 0; spin < (SPIN_CAP << 4); ++spin)
    if (__hip_atomic_load(c, __ATOMIC_RELAXED, __HIP_MEMORY_SCOPE_WORKGROUP) >= n) break;
  asm volatile("" ::: "memory");
}

template <int NS>
__device__ __forceinline__ void wide_load(rsrc_t r, int row0, int sw, int lane, u32x4 (&f)[NS][8]) {
  const int chunk = 16 * sw + (lane & 15);
  const int vo = chunk < 56 ? 8 * (lane >> 4) * 896 + chunk * 16 : OOB;
  u32x4 w[NS][8];
#pragma unroll
  for (int u = 0; u < NS; ++u)
#pragma unroll
    for (int j = 0; j < 8; ++j) w[u][j] = ld_nt(r, vo, (row0 + 32 * u + j) * 896);
#pragma unroll
  for (int u = 0; u < NS; ++u) transpose8(w[u], f[u]);
}
template <int NS>
__device__ __forceinline__ void wide_mma_publish(const u32x4 (&a)[NS], const u32x4 (&f)[NS][8], rsrc_t rw, int base, unsigned tag, int sw, int lane) {
  f32x4 acc[8];
#pragma unroll
  for (int i = 0; i < 8; ++i) acc[i] = mfma(a[0], f[0][i], f32x4{0.f, 0.f, 0.f, 0.f});
#pragma unroll
  for (int u = 1; u < NS; ++u)
#pragma unroll
    for (int i = 0; i < 8; ++i) acc[i] = mfma(a[u], f[u][i], acc[i]);
  const bool up = lane >= 32;
  const int vo = sw < 3 ? base + sw * 2048 + lane * 16 : ((lane & 8) ? OOB : base + 6144 + (lane >> 4) * 128 + (lane & 7) * 16);
  const int vo1 = vo == OOB ? OOB : vo + (sw < 3 ? 1024 : 512);
  st_dev16(rw, vo, asu(up ? acc[4][0] : acc[0][0]), asu(up ? acc[5][0] : acc[1][0]), tag);
  st_dev16(rw, vo1, asu(up ? acc[6][0] : acc[2][0]), asu(up ? acc[7][0] : acc[3][0]), tag);
}
__device__ __forceinline__ float reduce_half_line(rsrc_t rw, int base, int np, unsigned tag, int lane) {
  const int o = base + (lane >> 2) * 7168 + (lane & 3) * 16;
  const int vo[4] = {o, o + 16 * 7168, o + 32 * 7168, 48 + (lane >> 2) < np ? o + 48 * 7168 : OOB};
  u32x4 g[4];
  sweep16<4>(rw, vo, tag, g);
  float t = tr32((asf(g[0][0]) + asf(g[1][0])) + (asf(g[2][0]) + asf(g[3][0])), (asf(g[0][2]) + asf(g[1][2])) + (asf(g[2][2]) + asf(g[3][2])));
  t = tr16(t, t);
  t += dpp<X8>(t);
  return t + dpp<R4>(t);
}

__device__ __forceinline__ float kdot(const u32x4 k, const u32x4 q) { return dot2(k[3], q[3], dot2(k[2], q[2], dot2(k[1], q[1], dot2(k[0], q[0], 0.f)))); }
__device__ __forceinline__ float tile_softmax(float d0, float d1, float d2, float d3, float& m, float& l, float (&acc)[8], int lane) {
  const bool b8 = lane & 8, b4 = lane & 4;
  const float e0 = (b8 ? d2 : d0) + dpp<X8>(b8 ? d0 : d2), e1 = (b8 ? d3 : d1) + dpp<X8>(b8 ? d1 : d3);
  float s = (b4 ? e1 : e0) + dpp<X7>(b4 ? e0 : e1);
  s += dpp<X2>(s);
  s += dpp<X1>(s);
  s = rbf(tr16(s, s));
  if (__any(s > m)) {
    float t = fmaxf(s, dpp<X7>(s)); t = fmaxf(t, dpp<X8>(t)); t = mx32(t);
    const float al = ex2((m - t) * SC2);
    l *= al;
#pragma unroll
    for (int i = 0; i < 8; ++i) acc[i] *= al;
    m = t;
  }
  const float p = ex2((s - m) * SC2);
  l += p;
  return p;
}
template <int J> __device__ __forceinline__ void pv_row(const u32x4 v, float p, float (&acc)[8]) {
  const float pj = dpp<0x150 + 4 * J>(p);
#pragma unroll
  for (int i = 0; i < 4; ++i) { acc[2 * i] = fmaf(pj, blo(v[i]), acc[2 * i]); acc[2 * i + 1] = fmaf(pj, bhi(v[i]), acc[2 * i + 1]); }
}

constexpr int L_CNT = 0, L_TAG = 16, L_Q = 64, L_P = 1024, L_A = 5120, L_ST = 5376, L_O = 7168, L_ML = 11264, L_END = 11520;

__global__ void __launch_bounds__(512, 2)
k_block(const u16* __restrict__ x, const u16* __restrict__ Kc, const u16* __restrict__ Vc, const u16* __restrict__ wq, const u16* __restrict__ wo,
        const u16* __restrict__ wg, const u16* __restrict__ wu, const u16* __restrict__ wd, const u16* __restrict__ cv,
        const u16* __restrict__ wm, u16* __restrict__ out, unsigned char* ws) {
  __shared__ __attribute__((aligned(16))) unsigned char smem[L_END];
  LAS unsigned char* L = (LAS unsigned char*)smem;
  LAS unsigned* cnt = (LAS unsigned*)(L + L_CNT);
  const int tid = threadIdx.x, lane = tid & 63, wave = __builtin_amdgcn_readfirstlane(tid >> 6);
  const int bx = blockIdx.x & 7, br = blockIdx.x >> 3;
  const int pair = br, split = (bx + br) & 7;
  const int ridx = br * 2 + (split & 1);
  if (tid < 2) cnt[tid] = 0u;
  asm volatile("" :: "s"(x), "s"(Kc), "s"(Vc), "s"(wq), "s"(wo), "s"(wg), "s"(wu), "s"(wd), "s"(cv), "s"(wm), "s"(out), "s"(ws));
  BAR();
  const rsrc_t rw = mkr(ws, W_END);
  STAMP_INIT();

  if (wave < 4) {
    STAMP(0);
    const rsrc_t rk = mkr(Kc + ((size_t)pair * 8192 + split * 1024) * 256, 524288);
    const rsrc_t rv = mkr(Vc + ((size_t)pair * 8192 + split * 1024) * 256, 524288);
    const int vo = lane * 16 + wave * 1024;
    if (split >= 6 && ridx < 14) lds_wait(cnt + 1, 4);
    u32x4 k[6][4], v[6][4];
#define ISSUE(buf, r, t, W) _Pragma("unroll") for (int j = 0; j < 4; ++j) { if (W) asm volatile("s_waitcnt vmcnt(" PACE ")" ::: "memory"); buf[j] = ld_nt(r, vo, (t) * 16384 + j * 4096); SB(); }
    ISSUE(k[0], rk, 0, false) ISSUE(v[0], rv, 0, true)
#pragma unroll
    for (int t = 1; t < 6; ++t) { ISSUE(k[t], rk, t, true) ISSUE(v[t], rv, t, true) }
    STAMP(1);
    BAR();
    STAMP(2);
    const u32x4 q = *(const LAS u32x4*)(L + L_Q + (lane & 31) * 16);
    float m = -INFINITY, l = 0.f, acc[8];
#pragma unroll
    for (int i = 0; i < 8; ++i) acc[i] = 0.f;
#define KROLL(j, t) float d##j = kdot(k[0][j], q); asm volatile("" : "+v"(d##j)); SB(); k[0][j] = ld_nt(rk, vo, (t) * 16384 + j * 4096); SB();
#define VROLL(j, t) pv_row<j>(v[0][j], p, acc); PIN8(acc); SB(); v[0][j] = ld_nt(rv, vo, (t) * 16384 + j * 4096); SB();
#define ROLL(t) { KROLL(0, t) KROLL(1, t) KROLL(2, t) KROLL(3, t) const float p = tile_softmax(d0, d1, d2, d3, m, l, acc, lane); VROLL(0, t) VROLL(1, t) VROLL(2, t) VROLL(3, t) }
#define TILE(b) { const float p = tile_softmax(kdot(k[b][0], q), kdot(k[b][1], q), kdot(k[b][2], q), kdot(k[b][3], q), m, l, acc, lane); \
      pv_row<0>(v[b][0], p, acc); pv_row<1>(v[b][1], p, acc); pv_row<2>(v[b][2], p, acc); pv_row<3>(v[b][3], p, acc); PIN8(acc); SB(); }
    ROLL(6) TILE(1) TILE(2) ROLL(7) TILE(3) TILE(4) ROLL(8) TILE(5)
    STAMP(3);
#pragma nounroll
    for (int t = 9; t < 32; ++t) ROLL(t)
    TILE(0)
    l = wave_sum(l) * 0.125f;
    {
      f32x4 o = {tr32(acc[0], acc[4]), tr32(acc[1], acc[5]), tr32(acc[2], acc[6]), tr32(acc[3], acc[7])};
      *(LAS f32x4*)(L + L_O + wave * 1024 + (lane & 31) * 32 + (lane >> 5) * 16) = o;
      if (lane == 0) *(LAS f32x2*)(L + L_ML + wave * 8) = f32x2{m, l};
    }
    STAMP(4);
    BAR();
    if (wave < 2) {
      const f32x4 ml0 = *(const LAS f32x4*)(L + L_ML), ml1 = *(const LAS f32x4*)(L + L_ML + 16);
      const LAS f32x2* po = (const LAS f32x2*)(L + L_O) + tid;
      const f32x2 o0 = po[0], o1 = po[128], o2 = po[256], o3 = po[384];
      const unsigned tag = *(const LAS unsigned*)(L + L_TAG);
      const float M = fmaxf(fmaxf(ml0[0], ml0[2]), fmaxf(ml1[0], ml1[2]));
      const float w0 = ex2((ml0[0] - M) * SC2), w1 = ex2((ml0[2] - M) * SC2), w2 = ex2((ml1[0] - M) * SC2), w3 = ex2((ml1[2] - M) * SC2);
      const f32x2 o = (w0 * o0 + w1 * o1) + (w2 * o2 + w3 * o3);
      const int base = W_A + (pair * 8 + split) * 2560 + (tid >> 5) * 640;
      st_dev16(rw, base + (tid & 31) * 16, asu(o[0]), asu(o[1]), tag);
      st_dev16(rw, (tid & 31) == 0 ? base + 512 : OOB, asu(M), asu((w0 * ml0[1] + w1 * ml0[3]) + (w2 * ml1[1] + w3 * ml1[3])), tag);
      STAMP(5);
    }
    STAMP_FLUSH();
    return;
  }

  __builtin_amdgcn_s_setprio(3);
  const int sw = wave - 4;
  LAS float* P = (LAS float*)(L + L_P);
  const int le = lane & 3, lb = (lane >> 2) & 1, ls = (lane >> 3) & 1, kg = lane >> 4;
  const bool roleQ = split < 2;
  const bool roleS1 = split == 2 || split == 3;
  const int fi = br - (split & 1);
  const bool roleS3 = (split == 4 || split == 5) && fi >= 0 && fi < 28;
  const bool roleM = split >= 6 && ridx < 42;
  const int modb = W_MOD + bx * MOD_REP;
  unsigned ev = __builtin_amdgcn_raw_buffer_load_b32(rw, W_E, 0, 0);
  unsigned e0, tag;
#define GET_TAG() do { asm volatile("" : "+v"(ev)); e0 = __builtin_amdgcn_readfirstlane(ev); \
    tag = (e0 ^ (e0 >> 16)) * 0x7feb352du; tag = (tag ^ (tag >> 15)) * 0x846ca68bu; tag = (tag ^ (tag >> 16)) + 0x9e3779b9u; } while (0)
  u32x4 f[2][8];

  if (roleM || roleQ) {
    const rsrc_t rm = roleM ? mkr(wm, D * 6 * D * 2) : mkr(wq, D * 4096 * 2);
    const rsrc_t ra = mkr(roleM ? cv : x, 2 * D * 2);
    const int ldb = roleM ? 6 * D * 2 : 8192;
    const int kl = 32 * ls + 8 * kg + 2 * le;
    unsigned ap[7], own[2];
#pragma unroll
    for (int u = 0; u < 2; ++u) own[u] = __builtin_amdgcn_raw_buffer_load_b32(ra, (u == 1 && sw == 3) ? OOB : (lb * D + 64 * (2 * sw + u) + kl) * 2, 0, 0);
    SB();
    u32x4 w[2][8];
    {
      const int vo = (32 * ls + 8 * kg) * ldb + ridx * 128 + (lane & 7) * 16;
#pragma unroll
      for (int u = 0; u < 2; ++u)
#pragma unroll
        for (int j = 0; j < 8; ++j) {
          const bool dead = (u == 1) && (sw == 3);
          w[u][j] = ld_nt(rm, dead ? OOB : vo, dead ? 0 : (64 * (2 * sw + u) + j) * ldb);
        }
    }
    SB();
    u32x4 a[2];
    if (roleM) {
      lds_arrive(cnt + 1, lane);
      STAMP(24);
#pragma unroll
      for (int u = 0; u < 2; ++u) { const float c0 = blo(own[u]), c1 = bhi(own[u]); a[u] = quad_gather(pk(c0 * sigm(c0), c1 * sigm(c1))); }
      GET_TAG();
      asm volatile("" : "+v"(a[0]), "+v"(a[1]));
      SB();
      transpose8(w[0], f[0]);
      transpose8(w[1], f[1]);
    } else {
#pragma unroll
      for (int u = 0; u < 7; ++u) ap[u] = __builtin_amdgcn_raw_buffer_load_b32(ra, (lb * D + 64 * u + kl) * 2, 0, 0);
      float s = 0.f;
#pragma unroll
      for (int u = 0; u < 7; ++u) s += blo(ap[u]) + bhi(ap[u]);
      const float mean = batch_sum(s) * (1.f / D);
      s = 0.f;
#pragma unroll
      for (int u = 0; u < 7; ++u) { const float d0 = blo(ap[u]) - mean, d1 = bhi(ap[u]) - mean; s += d0 * d0 + d1 * d1; }
      const float sd = rbf(sqrtf(rbf(rbf(batch_sum(s) * (1.f / D)) + 1e-5f))), mb = rbf(mean);
      float ln[4];
#pragma unroll
      for (int u = 0; u < 2; ++u) { ln[2 * u] = rbf(rbf(blo(own[u]) - mb) / sd); ln[2 * u + 1] = rbf(rbf(bhi(own[u]) - mb) / sd); }
      GET_TAG();
      transpose8(w[0], f[0]);
      transpose8(w[1], f[1]);
      PIN8(f[0]); PIN8(f[1]);
      asm volatile("" : "+v"(ln[0]), "+v"(ln[1]), "+v"(ln[2]), "+v"(ln[3]));
      STAMP(9);
      const int o = modb + (lb * 2688 + 128 * sw + kl) * 4;
      const int vo[4] = {o, o + D * 4, sw == 3 ? OOB : o + 256, sw == 3 ? OOB : o + 256 + D * 4};
      unsigned g[4];
      sweep8<4, 1>(rw, vo, tag, g);
      STAMP(10);
#pragma unroll
      for (int u = 0; u < 2; ++u) {
        const unsigned c1 = pk(1.f + blo(g[2 * u + 1]), 1.f + bhi(g[2 * u + 1]));
        const unsigned t = pk(ln[2 * u] * blo(c1), ln[2 * u + 1] * bhi(c1));
        a[u] = quad_gather(pk(blo(t) + blo(g[2 * u]), bhi(t) + bhi(g[2 * u])));
      }
    }
    col_mma<false>(a, f, P, sw, lane);
    lds_arrive(cnt, lane);
    if (sw == 0) {
      lds_wait(cnt, 4);
      const LAS f32x2* p = (const LAS f32x2*)P + lane;
      const f32x2 s = ((p[0] + p[64]) + (p[128] + p[192])) + ((p[256] + p[320]) + (p[384] + p[448]));
      const u32x2 gr = {pk(s[0], s[1]), tag};
      const int vo = (roleM ? W_MOD + (lane >> 5) * 1344 * 8 : W_Q + (lane >> 5) * 2048 * 8) + ridx * 256 + (lane & 31) * 8;
      const int rep = roleM ? MOD_REP : Q_REP;
#pragma unroll
      for (int r = 0; r < 8; ++r) __builtin_amdgcn_raw_buffer_store_b64(gr, rw, vo, r * rep, AUX_SC1);
      if (roleM) STAMP(8); else STAMP(11);
    }
  } else if (roleS1) {
    wide_load<2>(mkr(wo, 4096 * D * 2), ridx * 64, sw, lane, f);
    GET_TAG();
  } else if (roleS3) {
    const int fg = 2 * fi + (split & 1);
    const rsrc_t r1 = mkr(wg, D * FF * 2), r2 = mkr(wu, D * FF * 2);
    const int vo = (32 * ls + 2 * kg) * (FF * 2) + fg * 64 + le * 16;
    u32x4 w[2][8];
#pragma unroll
    for (int u = 0; u < 2; ++u)
#pragma unroll
      for (int j = 0; j < 8; ++j) {
        const bool dead = (u == 1) && (sw == 3);
        const int so = dead ? 0 : (64 * (2 * sw + u) + 8 * (j >> 1) + (j & 1)) * (FF * 2);
        w[u][j] = ld_nt(r1, (dead || lb) ? OOB : vo, so) | ld_nt(r2, (dead || !lb) ? OOB : vo, so);
      }
    transpose8(w[0], f[0]);
    transpose8(w[1], f[1]);
    GET_TAG();
  } else {
    GET_TAG();
  }
  if (sw == 1) {
    const int o = W_Q + bx * Q_REP + pair * 1024 + lane * 8;
    const int vo[2] = {o, o + 512};
    unsigned g[2];
    sweep8<2, 4>(rw, vo, tag, g);
    *(LAS unsigned*)(L + L_Q + lane * 4) = g[0];
    *(LAS unsigned*)(L + L_Q + 256 + lane * 4) = g[1];
    if (lane == 0) *(LAS unsigned*)(L + L_TAG) = tag;
    STAMP(12);
  }
  asm volatile("" :: "s"(tag));
  BAR();

  if (roleS1) {
    PIN8(f[0]); PIN8(f[1]);
    const int b = sw >> 1, slot = (b * 16 + (ridx >> 2)) * 8 + (lane >> 3);
    const int o = W_A + slot * 2560 + (ridx & 3) * 640;
    const int vo[3] = {o + (sw & 1) * 256 + (lane & 7) * 16, o + (sw & 1) * 256 + 128 + (lane & 7) * 16, o + 512};
    BAR(); BAR();
    u32x4 g[3];
    sweep16<3>(rw, vo, tag, g);
    STAMP(16);
    const float ms = asf(g[2][0]);
    const float M = mx32(mx16(fmaxf(ms, dpp<X8>(ms))));
    const float w = ex2((ms - M) * SC2);
    float v0 = tr32(w * asf(g[0][0]), w * asf(g[1][0])), v1 = tr32(w * asf(g[0][2]), w * asf(g[1][2])), vl = w * asf(g[2][2]);
    vl = tr32(vl, vl);
    v0 = tr16(v0, v0); v1 = tr16(v1, v1); vl = tr16(vl, vl);
    v0 += dpp<X8>(v0); v1 += dpp<X8>(v1); vl += dpp<X8>(vl);
    const float inv = rcp(vl);
    if ((lane & 24) == 0) *(LAS unsigned*)(L + L_A + b * 128 + (sw & 1) * 64 + (lane >> 5) * 32 + (lane & 7) * 4) = pk(v0 * inv, v1 * inv);
    BAR();
    const u32x4 a[2] = {*(const LAS u32x4*)(L + L_A + lb * 128 + kg * 16), *(const LAS u32x4*)(L + L_A + lb * 128 + 64 + kg * 16)};
    wide_mma_publish<2>(a, f, rw, W_B + ridx * 7168, tag, sw, lane);
    STAMP(17);
  } else if (roleS3) {
    const int fg = 2 * fi + (split & 1);
    const int o = modb + (lb * 2688 + 3 * D + 128 * sw + 32 * ls + 8 * le + 2 * kg) * 4;
    const int vm[4] = {o, o + D * 4, sw == 3 ? OOB : o + 256, sw == 3 ? OOB : o + 256 + D * 4};
    unsigned gm[4];
    sweep8<4, 8>(rw, vm, tag, gm);
    const unsigned c1p[2] = {pk(1.f + blo(gm[1]), 1.f + bhi(gm[1])), pk(1.f + blo(gm[3]), 1.f + bhi(gm[3]))};
    const int ox = W_X + bx * X_REP + (lb * 56 + 16 * sw + 4 * ls + kg) * 128 + le * 16;
    const int vx[2] = {ox, sw == 3 ? OOB : ox + 1024};
    const LAS f32x2* pp = (const LAS f32x2*)(P + ls * 512 + lb * 64 + 8 * kg + 2 * le);
    f32x2 Gc[2], Gs[2];
    u32x4 fd[1][8];
#pragma nounroll
    for (int pass = 0; pass < 3; ++pass) {
      unsigned hi[2], lo[2];
      float s1 = 0.f, s2 = 0.f;
      if (pass == 2) {
        BAR(); BAR();
        wide_load<1>(mkr(wd, FF * D * 2), fg * 32, sw, lane, fd);
        PIN8(fd[0]);
        u32x4 g[2];
        sweep16<2>(rw, vx, tag, g);
        STAMP(20);
#pragma unroll
        for (int u = 0; u < 2; ++u) {
          const float x0 = asf(g[u][0]), x1 = asf(g[u][2]), u0 = x0 * blo(c1p[u]), u1 = x1 * bhi(c1p[u]);
          hi[u] = pk(u0, u1); lo[u] = pk(u0 - blo(hi[u]), u1 - bhi(hi[u]));
          s1 += x0 + x1; s2 += x0 * x0 + x1 * x1;
        }
      } else {
        hi[0] = pass == 0 ? c1p[0] : gm[0]; hi[1] = pass == 0 ? c1p[1] : gm[2]; lo[0] = 0u; lo[1] = 0u;
      }
      u32x4 a[2];
#pragma unroll
      for (int u = 0; u < 2; ++u) { const u32x4 h = quad_gather(hi[u]), l = quad_gather(lo[u]); a[u] = (lane & 1) ? l : h; }
      col_mma<true>(a, f, P, sw, lane);
      s1 = batch_sum(s1); s2 = batch_sum(s2);
      if ((lane & 59) == 0) *(LAS f32x2*)(L + L_ST + lb * 32 + sw * 8) = f32x2{s1, s2};
      if (pass == 2) BAR(); else { lds_arrive(cnt, lane); lds_wait(cnt, 8 * pass + 4); }
      f32x2 G[2];
#pragma unroll
      for (int i = 0; i < 2; ++i) {
        G[i] = (pp[16 * i] + pp[16 * i + 64]) + (pp[16 * i + 128] + pp[16 * i + 192]);
        G[i][0] += dpp<X8>(G[i][0]); G[i][1] += dpp<X8>(G[i][1]);
      }
      if (pass == 2) {
        const f32x4 t0 = *(const LAS f32x4*)(L + L_ST + lb * 32), t1 = *(const LAS f32x4*)(L + L_ST + lb * 32 + 16);
        const float mean = ((t0[0] + t0[2]) + (t1[0] + t1[2])) * (1.f / D);
        const float var = fmaxf(((t0[1] + t0[3]) + (t1[1] + t1[3])) * (1.f / D) - mean * mean, 0.f);
        const float rs = rcp(rbf(__builtin_amdgcn_sqrtf(rbf(rbf(var) + 1e-5f)))), mb = rbf(mean);
        const f32x2 gg = rs * (G[0] - mb * Gc[0]) + Gs[0], uu = rs * (G[1] - mb * Gc[1]) + Gs[1];
        const unsigned gp = pk(gg[0], gg[1]), up = pk(uu[0], uu[1]), sp = pk(sigm(blo(gp)), sigm(bhi(gp)));
        const u32x4 ad[1] = {quad_gather(pk(blo(sp) * blo(up), bhi(sp) * bhi(up)))};
        wide_mma_publish<1>(ad, fd, rw, W_D + fg * 7168, tag, sw, lane);
        STAMP(21);
      } else {
        if (pass == 0) { Gc[0] = G[0]; Gc[1] = G[1]; } else { Gs[0] = G[0]; Gs[1] = G[1]; }
        lds_arrive(cnt, lane); lds_wait(cnt, 8 * pass + 8);
      }
    }
  } else if (roleQ && ridx < 56 && sw < 2) {
    const int pw = ridx >> 4, g = pw < 3 ? (ridx >> 1) & 3 : ridx & 3, j = pw < 3 ? (ridx >> 3) & 1 : (ridx >> 2) & 1;
    const int cq = 4 * pw + (pw < 3 ? 2 * (ridx & 1) : 0) + sw, pos = 2 * (g >> 1) + j, b = g & 1;
    const int col = 32 * cq + 8 * le + 2 * pos;
    const unsigned xp = *(const unsigned*)(x + b * D + col);
    const int vo[2] = {modb + (b * 2688 + 2 * D + col) * 4, modb + (b * 2688 + 5 * D + col) * 4};
    unsigned gm[2];
    sweep8<2, 8>(rw, vo, tag, gm);
    const bool up = lane >= 32;
    const float xe = up ? bhi(xp) : blo(xp), ga = up ? bhi(gm[0]) : blo(gm[0]), gb = up ? bhi(gm[1]) : blo(gm[1]);
    BAR(); BAR();
    const float s = reduce_half_line(rw, W_B + ridx * 128 + sw * 64, 64, tag, lane);
    STAMP(18);
    const float x1 = rbf(xe + rbf(ga * rbf(s)));
    const auto xx = __builtin_amdgcn_permlane32_swap(asu(x1), asu(x1), false, false);
    st_dev16(rw, up ? OOB : W_X + (lane >> 2) * X_REP + (b * 56 + 4 * cq + pos) * 128 + le * 16, xx[0], xx[1], tag);
    STAMP(19);
    const float s2 = reduce_half_line(rw, W_D + ridx * 128 + sw * 64, 56, tag, lane);
    STAMP(22);
    const float y = x1 + rbf(gb * rbf(s2));
    const auto yy = __builtin_amdgcn_permlane32_swap(asu(y), asu(y), false, false);
    if (lane < 4) *(unsigned*)(out + b * D + col) = pk(asf(yy[0]), asf(yy[1]));
    if (blockIdx.x == 0 && tid == 256) __builtin_amdgcn_raw_buffer_store_b32(e0 + 1u, rw, W_E, 0, AUX_SC1);
    STAMP(23);
  }
  STAMP_FLUSH();
}

extern "C" void kernel_launch(void* const* d_in, const int* in_sizes, int n_in, void* d_out, int out_size, void* d_ws, size_t ws_size,
                              hipStream_t stream) {
  k_block<<<256, 512, 0, stream>>>((const u16*)d_in[0], (const u16*)d_in[1], (const u16*)d_in[2], (const u16*)d_in[3], (const u16*)d_in[4],
                                   (const u16*)d_in[6], (const u16*)d_in[7], (const u16*)d_in[8], (const u16*)d_in[9], (const u16*)d_in[10],
                                   (u16*)d_out, (unsigned char*)d_ws);
}
```

```cpp
#include <hip/hip_runtime.h>
#include <stdint.h>

typedef unsigned short u16;
typedef unsigned long long u64;
typedef unsigned u32x4 __attribute__((ext_vector_type(4)));
typedef unsigned u32x2 __attribute__((ext_vector_type(2)));
typedef float f32x4 __attribute__((ext_vector_type(4)));
typedef float f32x2 __attribute__((ext_vector_type(2)));
typedef __bf16 bf16x2 __attribute__((ext_vector_type(2)));
typedef __bf16 bf16x8 __attribute__((ext_vector_type(8)));
typedef __amdgpu_buffer_rsrc_t rsrc_t;
#define LAS __attribute__((address_space(3)))

#define TCON "i"
#define DLY_A 0
#define DLY_B 0
#define DLY_W 3
#define DLY_D 0
#define TGAP 2
#define DLY_Q 0
#define DLY_T 0
#define TA 56
#define TAU 56
#define TSTEP 40
#define S3LEAD 0
#define QHOLD 3
#define QSLEEP 640
#define XFLAG 96
#define LAG0 (TSTEP + 1)
#define LAGB 9999
#define TSTEP2 110
#define TSTEP3 200
#define BETA 5
#define PACE0 "5"
#define NOW 1
#define SLOTK 340
#define TEND 52
#define NPRE 12
#define OUTAUX 16
#define SLP_R 0
#define SLP_A 0
#define SLP_X 0
#define WARM 2
#define MAP 3
#define FIRSTN 6
#define GEOBITS 1 << 15, 1 << 10, 1 << 12, 1 << 9, 1 << 13, 1 << 11, 1 << 14, 1 << 16, 1 << 17, 1 << 18
constexpr int D = 448, FF = 1792;
constexpr int AUX_NT = 2, AUX_SC1 = 16;
constexpr int OOB = 0x40000000;
constexpr unsigned SPIN_CAP = 1u << 18;
constexpr float SC2 = 0.0625f * 1.4426950408889634f;

constexpr int W_E = 0;
constexpr int MOD_REP = 2 * 1344 * 8, Q_REP = 2 * 2048 * 8, X_REP = 2 * 56 * 128, VOTE_REP = 512;
constexpr int W_MOD = 4096;
constexpr int W_Q = W_MOD + 8 * MOD_REP;
constexpr int W_VOTE = W_Q + 2 * Q_REP;
constexpr int W_POS = W_VOTE + 8 * VOTE_REP;
constexpr int W_ML = W_POS + 9216;
constexpr int W_X = 1 << 20;
constexpr int W_A = W_X + 8 * X_REP;
constexpr int W_PROBE = 0x1c0000;
constexpr int W_B = 2 << 20;
constexpr int W_D = W_B + 64 * 7168;
constexpr int W_END = 6 << 20;
static_assert(W_ML + 4096 <= (1 << 21) && W_A + 256 * 2048 <= W_PROBE && W_D + 56 * 7168 <= (3 << 20), "workspace map");

#define STAMP(i) do {} while (0)
#define STAMP_VAL(i, v) do {} while (0)
#define STAMP_INIT() do {} while (0)
#define STAMP_FLUSH() do {} while (0)

__device__ __forceinline__ float asf(unsigned u) { return __builtin_bit_cast(float, u); }
__device__ __forceinline__ unsigned asu(float f) { return __builtin_bit_cast(unsigned, f); }
__device__ __forceinline__ float blo(unsigned u) { return asf(u << 16); }
__device__ __forceinline__ float bhi(unsigned u) { return asf(u & 0xffff0000u); }
__device__ __forceinline__ unsigned pk(float lo, float hi) {
  bf16x2 v = {(__bf16)lo, (__bf16)hi};
  return __builtin_bit_cast(unsigned, v);
}
__device__ __forceinline__ float rbf(float f) { return blo(pk(f, f)); }
__device__ __forceinline__ float rcp(float f) { return __builtin_amdgcn_rcpf(f); }
__device__ __forceinline__ float ex2(float f) { return __builtin_amdgcn_exp2f(f); }
__device__ __forceinline__ float sigm(float f) { return rcp(1.f + ex2(f * -1.4426950408889634f)); }
__device__ __forceinline__ float dot2(unsigned a, unsigned b, float c) {
  return __builtin_amdgcn_fdot2_f32_bf16(__builtin_bit_cast(bf16x2, a), __builtin_bit_cast(bf16x2, b), c, false);
}
template <int CTRL> __device__ __forceinline__ unsigned dppu(unsigned x) { return (unsigned)__builtin_amdgcn_mov_dpp((int)x, CTRL, 0xf, 0xf, true); }
template <int CTRL> __device__ __forceinline__ float dpp(float x) { return asf(dppu<CTRL>(asu(x))); }
constexpr int X1 = 0xB1, X2 = 0x4E, X7 = 0x141, X8 = 0x128, R4 = 0x124;
__device__ __forceinline__ u32x4 quad_gather(unsigned v) { return u32x4{dppu<0x00>(v), dppu<0x55>(v), dppu<0xAA>(v), dppu<0xFF>(v)}; }
__device__ __forceinline__ float tr16(float a, float b) {
  auto s = __builtin_amdgcn_permlane16_swap(asu(a), asu(b), false, false);
  return asf(s[0]) + asf(s[1]);
}
__device__ __forceinline__ float tr32(float a, float b) {
  auto s = __builtin_amdgcn_permlane32_swap(asu(a), asu(b), false, false);
  return asf(s[0]) + asf(s[1]);
}
__device__ __forceinline__ float mx16(float a) { auto s = __builtin_amdgcn_permlane16_swap(asu(a), asu(a), false, false); return fmaxf(asf(s[0]), asf(s[1])); }
__device__ __forceinline__ float mx32(float a) { auto s = __builtin_amdgcn_permlane32_swap(asu(a), asu(a), false, false); return fmaxf(asf(s[0]), asf(s[1])); }
__device__ __forceinline__ float wave_sum(float v) { v += dpp<X8>(v); v += dpp<X7>(v); v += dpp<X2>(v); v += dpp<X1>(v); v = tr16(v, v); return tr32(v, v); }
__device__ __forceinline__ float batch_sum(float v) { v += dpp<X1>(v); v += dpp<X2>(v); v += dpp<X8>(v); v = tr16(v, v); return tr32(v, v); }
__device__ __forceinline__ rsrc_t mkr(const void* p, unsigned bytes) { return __builtin_amdgcn_make_buffer_rsrc((void*)p, 0, bytes, 0x00020000); }
__device__ __forceinline__ u32x4 ld_nt(rsrc_t r, int vo, int so) { return __builtin_amdgcn_raw_buffer_load_b128(r, vo, so, AUX_NT); }
__device__ __forceinline__ void st_dev16(rsrc_t r, int vo, unsigned a, unsigned b, unsigned tag) {
  __builtin_amdgcn_raw_buffer_store_b128(u32x4{a, tag, b, tag}, r, vo, 0, AUX_SC1);
}
#define NAP(n) for (int i_ = 0; i_ < (n); ++i_) __builtin_amdgcn_s_sleep(3)
#define SB() __builtin_amdgcn_sched_barrier(0)
#define BAR() do { asm volatile("s_waitcnt lgkmcnt(0)" ::: "memory"); __builtin_amdgcn_s_barrier(); asm volatile("" ::: "memory"); } while (0)
#define PIN8(a) asm volatile("" : "+v"(a[0]), "+v"(a[1]), "+v"(a[2]), "+v"(a[3]), "+v"(a[4]), "+v"(a[5]), "+v"(a[6]), "+v"(a[7]))

__device__ __forceinline__ int near(int off, int side) {
  const int p = (off & 0x1fff) | ((off >> 13) << 14);
  return p | (((side ^ (p >> 21)) & 1) << 13);
}
template <int N, int SLEEP>
__device__ __forceinline__ void sweep16(rsrc_t r, const int (&vo)[N], unsigned tag, u32x4 (&g)[N]) {
  unsigned te[N];
#pragma unroll
  for (int i = 0; i < N; ++i) { te[i] = vo[i] == OOB ? 0u : tag; asm volatile("" : "+v"(te[i])); }
  unsigned spin = SPIN_CAP, bad, giveup;
#pragma nounroll
  do {
    if (SLEEP && spin != SPIN_CAP) __builtin_amdgcn_s_sleep(SLEEP);
    asm volatile("s_sub_u32 %0, %0, 1\n\ts_cselect_b32 %1, -1, 0" : "+s"(spin), "=s"(giveup) :: "memory", "scc");
    bad = 0;
#pragma unroll
    for (int i = 0; i < N; ++i) g[i] = __builtin_amdgcn_raw_buffer_load_b128(r, vo[i], 0, AUX_SC1);
#pragma unroll
    for (int i = 0; i < N; ++i) { bad |= __builtin_amdgcn_bitop3_b32(g[i][1], g[i][3], te[i], 0x7e); asm volatile("" : "+v"(bad)); }
  } while (__builtin_amdgcn_ballot_w64(bad > giveup) != 0);
  asm volatile("" ::: "memory");
}
template <int N, int SLEEP>
__device__ __forceinline__ void sweep8(rsrc_t r, const int (&vo)[N], unsigned tag, unsigned (&v)[N]) {
#pragma nounroll
  for (unsigned spin = 0; spin < SPIN_CAP; ++spin) {
    asm volatile("" ::: "memory");
    bool ok = true;
    u32x2 g[N];
#pragma unroll
    for (int i = 0; i < N; ++i) g[i] = __builtin_amdgcn_raw_buffer_load_b64(r, vo[i], 0, AUX_SC1);
#pragma unroll
    for (int i = 0; i < N; ++i) { ok = ok & ((g[i][1] == tag) | (vo[i] == OOB)); v[i] = g[i][0]; }
    if (__all(ok)) break;
    __builtin_amdgcn_s_sleep(SLEEP);
  }
  asm volatile("" ::: "memory");
}

__device__ __forceinline__ void transpose8(const u32x4 (&w)[8], u32x4 (&f)[8]) {
#pragma unroll
  for (int d = 0; d < 4; ++d)
#pragma unroll
    for (int e = 0; e < 4; ++e) {
      f[2 * d][e] = __builtin_amdgcn_perm(w[2 * e + 1][d], w[2 * e][d], 0x05040100u);
      f[2 * d + 1][e] = __builtin_amdgcn_perm(w[2 * e + 1][d], w[2 * e][d], 0x07060302u);
    }
}
__device__ __forceinline__ f32x4 mfma(u32x4 a, u32x4 b, f32x4 c) {
  return __builtin_amdgcn_mfma_f32_16x16x32_bf16(__builtin_bit_cast(bf16x8, a), __builtin_bit_cast(bf16x8, b), c, 0, 0, 0);
}

#define KEEP8(a) asm volatile("" :: "v"(a[0]), "v"(a[1]), "v"(a[2]), "v"(a[3]), "v"(a[4]), "v"(a[5]), "v"(a[6]), "v"(a[7]))
constexpr int P_DUMP = 4096;
template <bool GLU> __device__ __forceinline__ int col_slot(int sw, int lane) {
  const int ch = lane & 7, b = (lane >> 4) & 1;
  return ((lane >> 3) & 1) == (lane >> 5) ? sw * 1024 + (GLU ? (ch & 3) * 256 + b * 64 + (ch >> 2) * 4 : b * 512 + ch * 64) + (lane >> 5) * 8 : P_DUMP + lane * 4;
}
template <bool GLU> __device__ __forceinline__ constexpr int col_step(int i) { return (i >> 1) * 16 + (i & 1) * (GLU ? 128 : 4); }
template <bool GLU>
__device__ __forceinline__ void col_mma(const u32x4 (&a)[2], const u32x4 (&f)[2][8], LAS unsigned char* P, int sw, int lane) {
  LAS unsigned char* p = P + col_slot<GLU>(sw, lane);
  f32x4 acc[8];
#pragma unroll
  for (int i = 0; i < 8; ++i) acc[i] = mfma(a[1], f[1][i], mfma(a[0], f[0][i], f32x4{0.f, 0.f, 0.f, 0.f}));
#pragma unroll
  for (int i = 0; i < 8; ++i) *(LAS float*)(p + col_step<GLU>(i)) = acc[i][0];
  KEEP8(acc);
}
__device__ __forceinline__ f32x2 col_sum(const LAS unsigned char* p) {
  const f32x4 t = (*(const LAS f32x4*)p + *(const LAS f32x4*)(p + 1024)) + (*(const LAS f32x4*)(p + 2048) + *(const LAS f32x4*)(p + 3072));
  return f32x2{t[0], t[1]} + f32x2{t[2], t[3]};
}
__device__ __forceinline__ void lds_arrive(LAS unsigned* c, int lane) {
  asm volatile("s_waitcnt lgkmcnt(0)" ::: "memory");
  if (lane == 0) __hip_atomic_fetch_add(c, 1u, __ATOMIC_RELAXED, __HIP_MEMORY_SCOPE_WORKGROUP);
}
__device__ __forceinline__ void lds_wait(LAS unsigned* c, unsigned n) {
#pragma nounroll
  for (unsigned spin = 0; spin < (SPIN_CAP << 4); ++spin)
    if (__hip_atomic_load(c, __ATOMIC_RELAXED, __HIP_MEMORY_SCOPE_WORKGROUP) >= n) break;
  asm volatile("" ::: "memory");
}

template <int NS, bool SLOW>
__device__ __forceinline__ void wide_load(rsrc_t r, int row0, int sw, int lane, u32x4 (&w)[NS][8]) {
  const int chunk = 16 * sw + (lane & 15);
  const int vo = chunk < 56 ? 8 * (lane >> 4) * 896 + chunk * 16 : OOB;
#pragma unroll
  for (int u = 0; u < NS; ++u)
#pragma unroll
    for (int j = 0; j < 8; ++j) { if (SLOW) asm volatile("s_waitcnt vmcnt(1)" ::: "memory"); w[u][j] = ld_nt(r, vo, (row0 + 32 * u + j) * 896); if (SLOW) SB(); }
}
__device__ __forceinline__ int side_of(unsigned dec, int role, int idx) { return (idx ^ (dec >> (8 + ((role - (idx >> 1)) & 3))) ^ (dec >> 4)) & 1; }
__device__ __forceinline__ void wide_slots(int base, unsigned dec, int sw, int lane, int (&vo)[2]) {
#pragma unroll
  for (int j = 0; j < 2; ++j) {
    const int rho = sw < 3 ? sw * 16 + j * 8 + (lane >> 3) : 48 + j * 4 + (lane >> 4);
    vo[j] = (sw == 3 && (lane & 8)) ? OOB : near(base + rho * 128 + (lane & 7) * 16, side_of(dec, 0, rho));
  }
}
template <int NS>
__device__ __forceinline__ void wide_mma_publish(const u32x4 (&a)[NS], const u32x4 (&f)[NS][8], rsrc_t rw, const int (&vo)[2], unsigned tag, int lane) {
  f32x4 acc[8];
  const bool up = lane >= 32;
#pragma unroll
  for (int j = 0; j < 2; ++j) {
#pragma unroll
    for (int n = 0; n < 4; ++n) {
      const int i = 2 * j + (n & 1) + 4 * (n >> 1);
      acc[i] = mfma(a[0], f[0][i], f32x4{0.f, 0.f, 0.f, 0.f});
#pragma unroll
      for (int u = 1; u < NS; ++u) acc[i] = mfma(a[u], f[u][i], acc[i]);
    }
  }
  SB();
  st_dev16(rw, vo[0], asu(up ? acc[4][0] : acc[0][0]), asu(up ? acc[5][0] : acc[1][0]), tag);
  st_dev16(rw, vo[1], asu(up ? acc[6][0] : acc[2][0]), asu(up ? acc[7][0] : acc[3][0]), tag);
  KEEP8(acc);
}
__device__ __forceinline__ void half_line_slots(int base, int np, int side, int lane, int (&vo)[4]) {
#pragma unroll
  for (int j = 0; j < 4; ++j) vo[j] = 16 * j + (lane >> 2) < np ? near(base + (16 * j + (lane >> 2)) * 7168 + (lane & 3) * 16, side) : OOB;
}
__device__ __forceinline__ float reduce_half_line(rsrc_t rw, const int (&vo)[4], unsigned tag) {
  u32x4 g[4];
  sweep16<4, SLP_R>(rw, vo, tag, g);
  float t = tr32((asf(g[0][0]) + asf(g[1][0])) + (asf(g[2][0]) + asf(g[3][0])), (asf(g[0][2]) + asf(g[1][2])) + (asf(g[2][2]) + asf(g[3][2])));
  t = tr16(t, t);
  t += dpp<X8>(t);
  return t + dpp<R4>(t);
}
__device__ __forceinline__ int probe_side(const unsigned char* base) {
  int m = 0, bad = 0;
  u64 c0 = __builtin_amdgcn_s_memtime();
  const unsigned rnd = ((unsigned)c0 * 0x9e3779b1u) >> 8;
#pragma unroll
  for (int i = 0; i < 5; ++i) {
    const unsigned side = (rnd ^ (i >> 1)) & 1;
    const unsigned char* p = base + (((rnd >> 1) + 5 * i) & 31) * 16384 + side * 8192 + ((rnd >> 6) & 63) * 128;
    u64 c1; unsigned g;
    asm volatile("s_memtime %0\n\ts_waitcnt lgkmcnt(0)\n\ts_load_dword %2, %3, 0x0\n\ts_waitcnt lgkmcnt(0)\n\ts_memtime %1\n\ts_waitcnt lgkmcnt(0)"
                 : "=&s"(c0), "=&s"(c1), "=&s"(g) : "s"(p) : "memory");
    const int d = (int)(unsigned)(c1 - c0);
    if (i) { m += side ? d : -d; bad |= d < 330; }
  }
  return bad ? 0 : (m > 1000 ? 1000 : (m < -1000 ? -1000 : m));
}

__device__ __forceinline__ float kdot(const u32x4 k, const u32x4 q) { return dot2(k[3], q[3], dot2(k[2], q[2], dot2(k[1], q[1], dot2(k[0], q[0], 0.f)))); }
template <int CTRL> __device__ __forceinline__ float trd(bool bit, float a, float b) { return (bit ? b : a) + dpp<CTRL>(bit ? a : b); }
template <int B> __device__ __forceinline__ void pv_row(const u32x4 v, float p, float (&acc)[8]) {
  const float pj = dpp<0x150 + B>(p);
#pragma unroll
  for (int i = 0; i < 4; ++i) { acc[2 * i] = fmaf(pj, blo(v[i]), acc[2 * i]); acc[2 * i + 1] = fmaf(pj, bhi(v[i]), acc[2 * i + 1]); }
}
constexpr int GB[10] = {GEOBITS};
#define GM(x, b, k) (-(((x) >> (b)) & 1) & GB[k])
#define tile_off(t) (GM(t, 0, 5) ^ GM(t, 1, 6) ^ GM(t, 2, 7) ^ GM(t, 3, 8) ^ GM(t, 4, 9))
#define row_off(j) (GM(j, 0, 3) ^ GM(j, 1, 4))
#define lane_off(w, l) ((((l) & 31) << 4) | GM(l, 5, 0) | GM(w, 0, 1) | GM(w, 1, 2))
#define PACE_WAIT() asm volatile("s_cmp_gt_i32 %0, %1\n\ts_cbranch_scc1 2f\n\ts_cmp_gt_i32 %0, 0\n\ts_cbranch_scc1 1f\n\ts_waitcnt vmcnt(3)\n1:\n\ts_waitcnt vmcnt(4)\n\ts_branch 4f\n" \
    "2:\n\ts_cmp_gt_i32 %0, %3\n\ts_cbranch_scc1 4f\n\ts_cmp_gt_i32 %0, %2\n\ts_cbranch_scc1 3f\n\ts_waitcnt vmcnt(5)\n3:\n\ts_waitcnt vmcnt(6)\n4:" \
    :: "s"(lag), TCON(TSTEP), TCON(TSTEP2), TCON(TSTEP3) : "memory", "scc")
#define TRACE(t, now)
#define LAG(t) { const int now_ = (int)(unsigned)__builtin_amdgcn_s_memrealtime(), o_ = (now_ - (t) * TAU + tlead) | 1, r_ = __builtin_amdgcn_readfirstlane(refv); TRACE(t, now_) \
    lag = r_ ? o_ - r_ : LAG0; if (wave == 0) pace[0] = (t) < TEND ? o_ : 2; refv = pace[1]; SB(); }
constexpr int L_CNT = 0, L_TAG = 32, L_DEC = 40, L_Q = 64, L_A = 576, L_ST = 832, L_ML = 896, L_O = 1024, L_S = 5120, L_P = 9216, L_END = L_P + P_DUMP + 512;

__global__ void __launch_bounds__(512, 2)
k_block(const u16* __restrict__ x, const u16* __restrict__ Kc, const u16* __restrict__ Vc, const u16* __restrict__ wq, const u16* __restrict__ wo,
        const u16* __restrict__ wg, const u16* __restrict__ wu, const u16* __restrict__ wd, const u16* __restrict__ cv,
        const u16* __restrict__ wm, u16* __restrict__ out, unsigned char* ws
        ) {
  __shared__ __attribute__((aligned(16))) unsigned char smem[L_END];
  LAS unsigned char* L = (LAS unsigned char*)smem;
  LAS unsigned* cnt = (LAS unsigned*)(L + L_CNT);
  const int tid = threadIdx.x, lane = tid & 63, wave = __builtin_amdgcn_readfirstlane(tid >> 6);
  const int bx = blockIdx.x & 7, br = blockIdx.x >> 3;
  const int mem = bx >> 2, reg = (bx + br) & 3, ridx = br * 2 + mem;
  const int slice = MAP == 0 ? br * 8 + ((bx + br) & 7) : MAP == 1 ? bx * 32 + br : MAP == 2 ? ((br >> 2) * 8 + bx) * 4 + (br & 3) : ((br >> 2) * 8 + ((bx + (br >> 2)) & 7)) * 4 + (br & 3), pair = slice >> 3;
  volatile LAS int* pace = (volatile LAS int*)(L + L_CNT + 24);
  STAMP_INIT();
#define ARGS() asm volatile("" :: "s"(x), "s"(Kc), "s"(Vc), "s"(wq), "s"(wo), "s"(wg), "s"(wu), "s"(wd), "s"(cv), "s"(wm), "s"(out), "s"(ws))

  if (__builtin_expect(wave < 4, 1)) {
    const rsrc_t rw = mkr(ws, W_END);
    STAMP(27);
    const rsrc_t rk = mkr(Kc + (size_t)slice * 1024 * 256, 524288);
    const rsrc_t rv = mkr(Vc + (size_t)slice * 1024 * 256, 524288);
    const int vo = lane_off(wave, lane);
    const bool m1 = (XFLAG & 64) && reg == 3 && blockIdx.x < 56;
    u32x4 kb[NPRE][4];
#pragma unroll
    for (int n = 0; n < FIRSTN; ++n) { kb[n >> 2][n & 3] = ld_nt(rk, m1 ? OOB : vo, tile_off(n >> 2) ^ row_off(n & 3)); SB(); }
    BAR();
    const int tlead = reg == 2 ? S3LEAD : 0;
    const bool chain = reg == 0 || (reg == 3 && ridx < 14);
    const int fast = chain ? QHOLD : (reg == 2 ? 0 : 99);
    if (chain) lds_wait(cnt + 1, 4);
    if (m1) {
#pragma unroll
      for (int n = 0; n < FIRSTN; ++n) { kb[n >> 2][n & 3] = ld_nt(rk, vo, tile_off(n >> 2) ^ row_off(n & 3)); SB(); }
    }
#pragma unroll
    for (int n = FIRSTN; n < 4 * NPRE; ++n) {
      const int t = n >> 2, j = n & 3;
      if (t == QHOLD && j == 0 && chain) lds_wait(cnt + 2, 1);
      if (j == 0) { TRACE(t, (int)(unsigned)__builtin_amdgcn_s_memrealtime()) }
      asm volatile("s_cmp_lt_i32 %0, %1\n\ts_cbranch_scc1 1f\n\ts_waitcnt vmcnt(" PACE0 ")\n1:\n\ts_waitcnt vmcnt(7)" :: "s"(fast), "i"(t + 1) : "memory", "scc");
      kb[t][j] = ld_nt(rk, vo, tile_off(t) ^ row_off(j)); SB();
    }
    STAMP(1);
    BAR();
    STAMP(2);
    const u32x4 q = *(const LAS u32x4*)(L + L_Q + (lane & 31) * 16);
    const bool b1 = lane & 1, b4 = lane & 4, b8 = lane & 8;
    LAS float* sc = (LAS float*)(L + L_S + wave * 1024);
    int lag = LAG0, refv = 0;
    float d[4], e0, e1;
#define KROW(b, r, so, j) d[j] = kdot(kb[b][j], q); asm volatile("" : "+v"(d[j])); SB(); PACE_WAIT(); kb[b][j] = ld_nt(r, vo, (so) ^ row_off(j)); SB();
#define KROLL(e, b, r, so) KROW(b, r, so, 0) KROW(b, r, so, 1) KROW(b, r, so, 2) KROW(b, r, so, 3) e = trd<X7>(b4, trd<X8>(b8, d[0], d[2]), trd<X8>(b8, d[1], d[3]));
#define KTILE(t) trd<X7>(b4, trd<X8>(b8, kdot(kb[t][0], q), kdot(kb[t][2], q)), trd<X8>(b8, kdot(kb[t][1], q), kdot(kb[t][3], q)))
#define KPAIR(p, a, b) { float y_ = trd<X1>(b1, a, b); y_ += dpp<X2>(y_); sc[(lane & 45) + ((p) >> 2) * 64 + ((p) & 2) * 8 + ((p) & 1) * 2] = tr16(y_, y_); SB(); }
    LAG(NPRE) lag = LAGB; KROLL(e0, 0, rk, tile_off(NPRE)) KPAIR(1, KTILE(2), KTILE(3))
    KROLL(e1, 1, rk, tile_off(NPRE + 1)) KPAIR(0, e0, e1) KPAIR(2, KTILE(4), KTILE(5))
    LAG(NPRE + 2) lag = LAGB; KROLL(e0, 0, rk, tile_off(NPRE + 2)) KPAIR(3, KTILE(6), KTILE(7))
    KROLL(e1, 1, rk, tile_off(NPRE + 3)) KPAIR(NPRE / 2, e0, e1) KPAIR(4, KTILE(8), KTILE(9)) KPAIR(5, KTILE(10), KTILE(11))
    STAMP(3);
#pragma nounroll
    for (int p = NPRE / 2 + 1; p < 16; ++p) {
      const rsrc_t rn = p < 15 ? rk : rv;
      const int so = tile_off(2 * p + 2);
      LAG(2 * p + 2) KROLL(e0, 0, rn, so)
      if (p == 15 && (XFLAG & 32)) lag = LAGB;
      KROLL(e1, 1, rn, so ^ tile_off(1)) KPAIR(p, e0, e1)
    }
    STAMP(6);
    float p4[4], mw;
    {
      const unsigned s01 = pk(sc[lane], sc[lane + 64]), s23 = pk(sc[lane + 128], sc[lane + 192]);
      float m = fmaxf(fmaxf(blo(s01), bhi(s01)), fmaxf(blo(s23), bhi(s23)));
      m = fmaxf(m, dpp<X1>(m)); m = fmaxf(m, dpp<X2>(m)); m = fmaxf(m, dpp<X7>(m)); m = fmaxf(m, dpp<X8>(m));
      m = mx32(mx16(m));
      p4[0] = ex2((blo(s01) - m) * SC2); p4[1] = ex2((bhi(s01) - m) * SC2); p4[2] = ex2((blo(s23) - m) * SC2); p4[3] = ex2((bhi(s23) - m) * SC2);
      const float l = wave_sum((p4[0] + p4[1]) + (p4[2] + p4[3]));
      *(LAS f32x2*)(L + L_ML + wave * 8) = f32x2{m, l};
      lds_arrive(cnt + 4, lane);
      mw = m;
    }
    float acc[8];
#pragma unroll
    for (int i = 0; i < 8; ++i) acc[i] = 0.f;
#define VROW(n, j, so) pv_row<4 * (j) + (n)>(kb[(n) & 1][j], pc, acc); PIN8(acc); SB(); PACE_WAIT(); kb[(n) & 1][j] = ld_nt(rv, vo, (so) ^ row_off(j)); SB();
#define VTILE(n, so) VROW(n, 0, so) VROW(n, 1, so) VROW(n, 2, so) VROW(n, 3, so)
#define VLAST(n) pv_row<(n)>(kb[(n) & 1][0], pc, acc); pv_row<4 + (n)>(kb[(n) & 1][1], pc, acc); pv_row<8 + (n)>(kb[(n) & 1][2], pc, acc); pv_row<12 + (n)>(kb[(n) & 1][3], pc, acc);
    float pc, pn = 0.f;
#pragma nounroll
    for (int i = 0;; ++i) {
      if ((i & 1) == 0) {
        const auto pp = __builtin_amdgcn_permlane16_swap(asu(p4[0]), asu(p4[0]), false, false);
        pc = asf(pp[0]); pn = asf(pp[1]);
        p4[0] = p4[1]; p4[1] = p4[2]; p4[2] = p4[3];
      } else pc = pn;
      const int so = tile_off(4 * i), sn = tile_off(4 * i + 4);
      LAG(4 * i + 34) VTILE(0, so ^ tile_off(2)) VTILE(1, so ^ tile_off(3))
      if (i == 7) break;
      LAG(4 * i + 36) VTILE(2, sn) VTILE(3, sn ^ tile_off(1))
    }
    const unsigned tag = __builtin_amdgcn_readfirstlane(*(const LAS unsigned*)(L + L_TAG));
    lds_wait(cnt + 3, 1);
    int va = lane >= 32 ? OOB : near(W_A + slice * 2048 + wave * 512 + lane * 16,
                                     side_of(__builtin_amdgcn_readfirstlane(*(const LAS unsigned*)(L + L_DEC)), 1, (pair & 15) * 4 + wave));
    float wsc = ex2((mw - *(const LAS float*)(L + L_DEC + 4)) * SC2);
    int lo_w = L_O + wave * 1024 + (lane & 31) * 32 + (lane >> 5) * 16, lo_r = L_O + wave * 256 + (lane & 31) * 8;
    asm volatile("" : "+v"(va), "+v"(wsc), "+v"(lo_w), "+v"(lo_r)); SB();
    VLAST(2) VLAST(3)
    *(LAS f32x4*)(L + lo_w) = wsc * f32x4{tr32(acc[0], acc[4]), tr32(acc[1], acc[5]), tr32(acc[2], acc[6]), tr32(acc[3], acc[7])};
    STAMP(4);
    BAR();
    {
      const LAS f32x2* po = (const LAS f32x2*)(L + lo_r);
      const f32x2 o = (po[0] + po[128]) + (po[256] + po[384]);
      st_dev16(rw, va, asu(o[0]), asu(o[1]), tag);
      STAMP(5);
    }
    STAMP_FLUSH();
    return;
  }

  if (tid < 264) cnt[tid - 256] = 0u;
  ARGS();
  BAR();
  const rsrc_t rw = mkr(ws, W_END);
  __builtin_amdgcn_s_setprio(3);
  const int sw = wave - 4;
  const int tsvc = (int)(unsigned)__builtin_amdgcn_s_memrealtime();
  LAS unsigned char* P = L + L_P;
  const int le = lane & 3, lb = (lane >> 2) & 1, ls = (lane >> 3) & 1, kg = lane >> 4;
  const int fg = ((((br >> 3) * 4 + (br & 3)) * 2 + mem) << 1) | ((br >> 2) & 1);
  const bool roleQ = reg == 0, roleS1 = reg == 1, roleS3 = reg == 2 && fg < 56, roleM = reg == 3 && ridx < 42;
  const int modb = W_MOD + bx * MOD_REP;
  unsigned ev = __builtin_amdgcn_raw_buffer_load_b32(rw, W_E, 0, 0);
  unsigned e0, tag;
#define GET_TAG() do { asm volatile("" : "+v"(ev)); e0 = __builtin_amdgcn_readfirstlane(ev); \
    tag = (e0 ^ (e0 >> 16)) * 0x7feb352du; tag = (tag ^ (tag >> 15)) * 0x846ca68bu; tag = (tag ^ (tag >> 16)) + 0x9e3779b9u; } while (0)
  NAP(roleQ ? DLY_Q : (roleM && ridx < 14 ? 0 : DLY_T));
  u32x4 f[2][8];
#define GU_LOAD(none) { const rsrc_t r1 = mkr(wg, NOW * D * FF * 2), r2 = mkr(wu, NOW * D * FF * 2); \
      const int vo = (32 * ls + 2 * kg) * (FF * 2) + fg * 64 + le * 16; \
      _Pragma("unroll") for (int u = 0; u < 2; ++u) _Pragma("unroll") for (int j = 0; j < 8; ++j) { \
          const bool dead = ((u == 1) && (sw == 3)) || (none); \
          const int so = dead ? 0 : (64 * (2 * sw + u) + 8 * (j >> 1) + (j & 1)) * (FF * 2); \
          w[u][j] = ld_nt(r1, (dead || lb) ? OOB : vo, so); w2[u][j] = ld_nt(r2, (dead || !lb) ? OOB : vo, so); } }
#define GU_MERGE() _Pragma("unroll") for (int j = 0; j < 16; ++j) w[j >> 3][j & 7] |= w2[j >> 3][j & 7];

  if (roleM || roleQ) {
    const rsrc_t rm = roleM ? mkr(wm, D * 6 * D * 2) : mkr(wq, D * 4096 * 2);
    const rsrc_t ra = mkr(roleM ? cv : x, 2 * D * 2);
    const int ldb = roleM ? 6 * D * 2 : 8192;
    const int kl = 32 * ls + 8 * kg + 2 * le;
    unsigned ap[7], own[2];
#pragma unroll
    for (int u = 0; u < 2; ++u) own[u] = __builtin_amdgcn_raw_buffer_load_b32(ra, (u == 1 && sw == 3) ? OOB : (lb * D + 64 * (2 * sw + u) + kl) * 2, 0, 0);
#pragma unroll
    for (int u = 0; u < 7; ++u) ap[u] = __builtin_amdgcn_raw_buffer_load_b32(ra, roleM ? OOB : (lb * D + 64 * u + kl) * 2, 0, 0);
    SB();
    u32x4 w[2][8];
    {
      const int vo = (32 * ls + 8 * kg) * ldb + ridx * 128 + (lane & 7) * 16;
#pragma unroll
      for (int u = 0; u < 2; ++u)
#pragma unroll
        for (int j = 0; j < 8; ++j) {
          const bool dead = (u == 1) && (sw == 3);
          w[u][j] = ld_nt(rm, dead ? OOB : vo, dead ? 0 : (64 * (2 * sw + u) + j) * ldb);
        }
    }
    SB();
    u32x4 a[2];
    const int vpub = (roleM ? W_MOD + (lane >> 5) * 1344 * 8 : W_Q + (lane >> 5) * 2048 * 8) + ridx * 256 + (lane & 31) * 8;
    const int rep = roleM ? MOD_REP : Q_REP;
    lds_arrive(cnt + 1, lane);
    if (roleM) {
      STAMP(24);
#pragma unroll
      for (int u = 0; u < 2; ++u) { const float c0 = blo(own[u]), c1 = bhi(own[u]); a[u] = quad_gather(pk(c0 * sigm(c0), c1 * sigm(c1))); }
      GET_TAG();
      asm volatile("" : "+v"(a[0]), "+v"(a[1]));
      SB();
      transpose8(w[0], f[0]);
      transpose8(w[1], f[1]);
    } else {
      float s = 0.f;
#pragma unroll
      for (int u = 0; u < 7; ++u) s += blo(ap[u]) + bhi(ap[u]);
      const float mean = batch_sum(s) * (1.f / D);
      s = 0.f;
#pragma unroll
      for (int u = 0; u < 7; ++u) { const float d0 = blo(ap[u]) - mean, d1 = bhi(ap[u]) - mean; s += d0 * d0 + d1 * d1; }
      const float sd = rbf(sqrtf(rbf(rbf(batch_sum(s) * (1.f / D)) + 1e-5f))), mb = rbf(mean);
      float ln[4];
#pragma unroll
      for (int u = 0; u < 2; ++u) { ln[2 * u] = rbf(rbf(blo(own[u]) - mb) / sd); ln[2 * u + 1] = rbf(rbf(bhi(own[u]) - mb) / sd); }
      GET_TAG();
      transpose8(w[0], f[0]);
      transpose8(w[1], f[1]);
      PIN8(f[0]); PIN8(f[1]);
      asm volatile("" : "+v"(ln[0]), "+v"(ln[1]), "+v"(ln[2]), "+v"(ln[3]));
      STAMP(9);
      const int o = modb + (lb * 2688 + 128 * sw + kl) * 4;
      const int vo[4] = {o, o + D * 4, sw == 3 ? OOB : o + 256, sw == 3 ? OOB : o + 256 + D * 4};
      unsigned g[4];
      sweep8<4, 1>(rw, vo, tag, g);
      STAMP(10);
#pragma unroll
      for (int u = 0; u < 2; ++u) {
        const unsigned c1 = pk(1.f + blo(g[2 * u + 1]), 1.f + bhi(g[2 * u + 1]));
        const unsigned t = pk(ln[2 * u] * blo(c1), ln[2 * u + 1] * bhi(c1));
        a[u] = quad_gather(pk(blo(t) + blo(g[2 * u]), bhi(t) + bhi(g[2 * u])));
      }
    }
    col_mma<false>(a, f, P, sw, lane);
    lds_arrive(cnt, lane);
    if (roleM) STAMP(31); else STAMP(14);
    if (sw == 0) {
      lds_wait(cnt, 4);
      if (!roleM) STAMP(15);
      const f32x2 cs = col_sum(P + lane * 16);
      const u32x2 gr = {pk(cs[0], cs[1]), tag};
      const int nrep = roleM ? (ridx < 14 ? 8 : 1) : 2;
#pragma nounroll
      for (int r = 0; r < nrep; ++r) __builtin_amdgcn_raw_buffer_store_b64(gr, rw, vpub, r * rep, AUX_SC1);
      lds_arrive(cnt + 2, lane);
      if (roleM) STAMP(8); else STAMP(11);
    }
  } else if (reg != 3) {
    u32x4 w[2][8], w2[2][8];
    if (roleS1) {
      wide_load<2, false>(mkr(wo, NOW * 4096 * D * 2), ridx * 64, sw, lane, w);
    } else {
      GU_LOAD(!roleS3)
    }
    SB();
    GET_TAG();
    if (!roleS1) { GU_MERGE() }
    transpose8(w[0], f[0]); transpose8(w[1], f[1]);
  } else {
    GET_TAG();
  }
  if (sw == 1) {
    for (int i = 0; i < 128 && (int)(unsigned)__builtin_amdgcn_s_memrealtime() - tsvc < QSLEEP; ++i) __builtin_amdgcn_s_sleep(4);
    const int o = W_Q + ((slice >> 2) & 1) * Q_REP + pair * 1024 + lane * 8;
    const int vo[2] = {o, o + 512};
    unsigned g[2];
    sweep8<2, 1>(rw, vo, tag, g);
    *(LAS unsigned*)(L + L_Q + lane * 4) = g[0];
    *(LAS unsigned*)(L + L_Q + 256 + lane * 4) = g[1];
    if (lane == 0) *(LAS unsigned*)(L + L_TAG) = tag;
    STAMP(12);
  }
  asm volatile("" :: "s"(tag));
  BAR();
  if (roleS1 && sw == 0) {
    const unsigned iod = (__builtin_amdgcn_s_getreg(20 | (3 << 11)) >> 2) & 1;
    const int vote = probe_side(ws + 2 * W_PROBE);
    __builtin_amdgcn_raw_buffer_store_b64(u32x2{(unsigned)((iod ? vote : -vote) + 1024) | (iod << 16), tag}, rw, lane < 8 ? W_VOTE + lane * VOTE_REP + ridx * 8 : OOB, 0, AUX_SC1);
    STAMP(25);
  }
  int pp = lane * 16;
  f32x2 Gc = {0.f, 0.f}, Gs = {0.f, 0.f};
  unsigned gm[4];
  auto prepass = [&]() {
#pragma unroll
    for (int pass = 0; pass < 2; ++pass) {
      u32x4 a[2];
#pragma unroll
      for (int u = 0; u < 2; ++u) a[u] = quad_gather(pass ? gm[2 * u] : pk(1.f + blo(gm[2 * u + 1]), 1.f + bhi(gm[2 * u + 1])));
      col_mma<true>(a, f, P, sw, lane);
      lds_arrive(cnt, lane); lds_wait(cnt, 8 * pass + 4);
      (pass ? Gs : Gc) = col_sum(P + pp);
      lds_arrive(cnt, lane); lds_wait(cnt, 8 * pass + 8);
    }
  };
  if (roleS3) {
    const int o = W_MOD + (lb * 2688 + 3 * D + 128 * sw + 32 * ls + 8 * le + 2 * kg) * 4;
    const int vm[4] = {o, o + D * 4, sw == 3 ? OOB : o + 256, sw == 3 ? OOB : o + 256 + D * 4};
    sweep8<4, 8>(rw, vm, tag, gm);
    prepass();
    Gc[0] *= -1.4426950408889634f; Gs[0] *= -1.4426950408889634f;
  }
  if (sw == 2) {
    for (int i = 0; i < 10; ++i) __builtin_amdgcn_s_sleep(32);
    const int vo[1] = {W_VOTE + bx * VOTE_REP + lane * 8};
    unsigned g[1];
    sweep8<1, 32>(rw, vo, tag, g);
    const float t = wave_sum((float)(int)((g[0] & 0xffffu) - 1024));
    unsigned dd = t < 0.f ? 16u : 0u;
#pragma unroll
    for (int c = 0; c < 4; ++c) dd |= (((unsigned)__builtin_amdgcn_readlane((int)g[0], 2 * ((1 - c) & 3)) >> 16) & 1u) << (8 + c);
    for (unsigned spin = 0; spin < SPIN_CAP && __hip_atomic_load(cnt + 4, __ATOMIC_RELAXED, __HIP_MEMORY_SCOPE_WORKGROUP) < 4; ++spin) __builtin_amdgcn_s_sleep(8);
    asm volatile("" ::: "memory");
    const f32x4 ml0 = *(const LAS f32x4*)(L + L_ML), ml1 = *(const LAS f32x4*)(L + L_ML + 16);
    const float M = fmaxf(fmaxf(ml0[0], ml0[2]), fmaxf(ml1[0], ml1[2]));
    const float Ls = (ex2((ml0[0] - M) * SC2) * ml0[1] + ex2((ml0[2] - M) * SC2) * ml0[3]) + (ex2((ml1[0] - M) * SC2) * ml1[1] + ex2((ml1[2] - M) * SC2) * ml1[3]);
    st_dev16(rw, lane == 0 ? W_ML + slice * 16 : OOB, asu(M), asu(Ls), tag);
    *(LAS u32x2*)(L + L_DEC) = u32x2{dd, asu(M)};
    lds_arrive(cnt + 3, lane);
    STAMP(13);
    STAMP_VAL(29, __builtin_amdgcn_readfirstlane((int)t)); STAMP_VAL(30, __builtin_amdgcn_readfirstlane(dd));
  }

  if (sw == 3) {
    const int vr = (((br >> 2) & 3) == 2 && lane == 0) ? W_POS + (((br >> 4) * 4 + (br & 3)) * 8 + bx) * 128 : OOB;
    unsigned last = ~0u;
#pragma nounroll
    for (int i = 0; i < 65536; ++i) {
      const int o = __builtin_amdgcn_readfirstlane(pace[0]);
      if (o == 2) break;
      const unsigned half = (((unsigned)__builtin_amdgcn_s_memrealtime() & 0xfffffu) * SLOTK) >> 16;
      if (o && half != last) {
        last = half;
        if ((half & 1) == 0) {
          __builtin_amdgcn_raw_buffer_store_b64(u32x2{(unsigned)o, tag}, rw, vr, 0, AUX_SC1);
          const u32x2 r = __builtin_amdgcn_raw_buffer_load_b64(rw, W_POS + 8192 + bx * 128, 0, AUX_SC1);
          if (r[1] == tag) pace[1] = (int)r[0];
        } else if (reg == 3 && ridx == 48 + ((half >> 1) & 15)) {
          const u32x2 g = __builtin_amdgcn_raw_buffer_load_b64(rw, W_POS + lane * 128, 0, AUX_SC1);
          const bool ok = g[1] == tag;
          float n = ok ? 1.f : 0.f, dv = ok ? (float)((int)g[0] - o) : 0.f;
          n += dpp<X8>(n); n = tr16(n, n); n = tr32(n, n); dv += dpp<X8>(dv); dv = tr16(dv, dv); dv = tr32(dv, dv);
          float slow = n > 0.f ? dv * rcp(n) : -1e9f;
          slow = fmaxf(slow, dpp<X1>(slow)); slow = fmaxf(slow, dpp<X2>(slow)); slow = fmaxf(slow, dpp<X7>(slow));
          n += dpp<X1>(n); n += dpp<X2>(n); n += dpp<X7>(n); dv += dpp<X1>(dv); dv += dpp<X2>(dv); dv += dpp<X7>(dv);
          dv *= rcp(n);
          dv += (slow - dv) * (BETA * 0.0625f);
          __builtin_amdgcn_raw_buffer_store_b64(u32x2{(unsigned)((o + (int)dv - TA) | 1), tag}, rw, (n > 40.f && lane < 8) ? W_POS + 8192 + lane * 128 : OOB, 0, AUX_SC1);
        }
        asm volatile("" ::: "memory");
      }
      __builtin_amdgcn_s_sleep(6);
    }
  }
  if (!(roleS1 || roleS3 || (roleQ && ridx < 56 && sw < 2))) { STAMP_FLUSH(); return; }
  for (unsigned spin = 0; spin < SPIN_CAP && __hip_atomic_load(cnt + 3, __ATOMIC_RELAXED, __HIP_MEMORY_SCOPE_WORKGROUP) == 0; ++spin) __builtin_amdgcn_s_sleep(8);
  asm volatile("" ::: "memory");
  const unsigned dec = __builtin_amdgcn_readfirstlane(*(const LAS unsigned*)(L + L_DEC));
  const int side = side_of(dec, reg, ridx);

  if (roleS1) {
    const int b = sw >> 1, slot = (b * 16 + (ridx >> 2)) * 8 + (lane >> 3);
    const int o = W_A + slot * 2048 + (ridx & 3) * 512 + (sw & 1) * 256 + (lane & 7) * 16;
    int vo[2] = {near(o, side), near(o + 128, side)};
    int vp[2];
    wide_slots(W_B + ridx * 7168, dec, sw, lane, vp);
    for (unsigned spin = 0; spin < SPIN_CAP && pace[0] != 2; ++spin) __builtin_amdgcn_s_sleep(8);
    const int vm[1] = {W_ML + slot * 16};
    u32x4 gl[1];
    sweep16<1, 8>(rw, vm, tag, gl);
    const float ms = asf(gl[0][0]);
    float w = ex2((ms - mx32(mx16(fmaxf(ms, dpp<X8>(ms))))) * SC2), vl = w * asf(gl[0][2]);
    vl += dpp<X8>(vl); vl = tr16(vl, vl);
    w *= rcp(tr32(vl, vl));
    int la_w = L_A + b * 128 + (sw & 1) * 64 + (lane >> 5) * 32 + (lane & 7) * 4 + (kg & 1) * 2, la_r = L_A + lb * 128 + kg * 16;
    PIN8(f[0]); PIN8(f[1]);
    asm volatile("" : "+v"(vo[0]), "+v"(vo[1]), "+v"(vp[0]), "+v"(vp[1]), "+v"(w), "+v"(la_w), "+v"(la_r));
    BAR(); BAR();
    NAP(DLY_A);
    u32x4 g[2];
    sweep16<2, SLP_A>(rw, vo, tag, g);
    STAMP(16);
    float v = tr16(tr32(w * asf(g[0][0]), w * asf(g[1][0])), tr32(w * asf(g[0][2]), w * asf(g[1][2])));
    v += dpp<X8>(v);
    *(LAS u16*)(L + la_w) = (u16)pk(v, v);
    BAR();
    const u32x4 a[2] = {*(const LAS u32x4*)(L + la_r), *(const LAS u32x4*)(L + la_r + 64)};
    wide_mma_publish<2>(a, f, rw, vp, tag, lane);
    STAMP(17);
  } else if (roleS3) {
    const int ox = W_X + bx * X_REP + (lb * 56 + 16 * sw + 4 * ls + kg) * 128 + le * 16;
    int vx[2] = {le == 3 ? OOB : near(ox, side), (sw == 3 || le == 3) ? OOB : near(ox + 1024, side)};
    int vp[2];
    wide_slots(W_D + fg * 7168, dec, sw, lane, vp);
    u32x4 wdn[1][8], fd[1][8];
    int l_st = le == 2 ? L_ST + lb * 32 + sw * 8 + (lane >> 5) * 4 : L_P + P_DUMP + lane * 4, l_sr = L_ST + lb * 32, l_pw = L_P + col_slot<true>(sw, lane);
    asm volatile("" : "+v"(vx[0]), "+v"(vx[1]), "+v"(vp[0]), "+v"(vp[1]), "+v"(Gc), "+v"(Gs), "+v"(l_st), "+v"(l_sr), "+v"(l_pw), "+v"(pp));
    BAR(); BAR();
    NAP(DLY_W);
    wide_load<1, false>(mkr(wd, FF * D * 2), fg * 32, sw, lane, wdn);
    transpose8(wdn[0], fd[0]);
    PIN8(fd[0]);
    STAMP(26);
    u32x4 g[2];
    sweep16<2, SLP_X>(rw, vx, tag, g);
    STAMP(20);
    f32x4 acc[8];
    const f32x4 z4 = {0.f, 0.f, 0.f, 0.f};
#define PW(i) *(LAS float*)(L + l_pw + col_step<true>(i)) = acc[i][0];
    const u32x4 a0 = {dppu<0x00>(g[0][0]), dppu<0x00>(g[0][2]), dppu<0x55>(g[0][0]), dppu<0x55>(g[0][2])};
    const float s1 = asf(g[0][0]) + asf(g[1][0]), s2 = asf(g[0][2]) + asf(g[1][2]);
    SB();
    acc[0] = mfma(a0, f[0][0], z4); SB();
    float st = tr32(s1, s2), su = st; asm volatile("" : "+v"(su)); SB();
    acc[1] = mfma(a0, f[0][1], z4); acc[2] = mfma(a0, f[0][2], z4); SB();
    st = tr16(st, su); SB();
    acc[3] = mfma(a0, f[0][3], z4); acc[4] = mfma(a0, f[0][4], z4); SB();
    st += dpp<X8>(st);
    *(LAS float*)(L + l_st) = st; SB();
    acc[5] = mfma(a0, f[0][5], z4); SB();
    u32x4 a1;
    a1[0] = dppu<0x00>(g[1][0]); a1[1] = dppu<0x00>(g[1][2]); SB();
    acc[6] = mfma(a0, f[0][6], z4); SB();
    a1[2] = dppu<0x55>(g[1][0]); a1[3] = dppu<0x55>(g[1][2]); SB();
    acc[7] = mfma(a0, f[0][7], z4); SB();
    BAR(); SB();
    const f32x4 t0 = *(const LAS f32x4*)(L + l_sr), t1 = *(const LAS f32x4*)(L + l_sr + 16); SB();
    acc[0] = mfma(a1, f[1][0], acc[0]); acc[1] = mfma(a1, f[1][1], acc[1]); acc[2] = mfma(a1, f[1][2], acc[2]); acc[3] = mfma(a1, f[1][3], acc[3]); SB();
    PW(0) PW(1) SB();
    acc[4] = mfma(a1, f[1][4], acc[4]); acc[5] = mfma(a1, f[1][5], acc[5]); SB();
    PW(2) PW(3) SB();
    acc[6] = mfma(a1, f[1][6], acc[6]); acc[7] = mfma(a1, f[1][7], acc[7]); SB();
    PW(4) PW(5)
    const f32x4 t2 = t0 + t1;
    f32x2 mv = (f32x2{t2[0], t2[1]} + f32x2{t2[2], t2[3]}) * (1.f / D);
    asm volatile("" : "+v"(mv)); SB();
    PW(6) PW(7) SB();
    KEEP8(acc);
    const unsigned mq = pk(mv[0], fmaxf(mv[1] - mv[0] * mv[0], 0.f));
    float sd = __builtin_amdgcn_sqrtf(rbf(bhi(mq) + 1e-5f));
    asm volatile("" : "+v"(sd)); SB();
    BAR(); SB();
    const LAS unsigned char* pr = P + pp;
    const f32x4 r0 = *(const LAS f32x4*)pr, r1 = *(const LAS f32x4*)(pr + 1024), r2 = *(const LAS f32x4*)(pr + 2048), r3 = *(const LAS f32x4*)(pr + 3072); SB();
    const float rs = rcp(rbf(sd));
    f32x2 sc2 = rs * f32x2{-1.4426950408889634f, 1.f}, off = Gs - (rs * blo(mq)) * Gc;
    asm volatile("" : "+v"(sc2), "+v"(off)); SB();
    const f32x4 r4 = (r0 + r1) + (r2 + r3);
    const f32x2 gv = sc2 * (f32x2{r4[0], r4[1]} + f32x2{r4[2], r4[3]}) + off;
    const float hh = gv[1] * rcp(1.f + ex2(gv[0])), hn = dpp<X8>(hh);
    const u32x4 ad[1] = {quad_gather(pk(ls ? hn : hh, ls ? hh : hn))};
    wide_mma_publish<1>(ad, fd, rw, vp, tag, lane);
    STAMP(21);
  } else if (roleQ && ridx < 56 && sw < 2) {
    const int pw = ridx >> 4, g = pw < 3 ? (ridx >> 1) & 3 : ridx & 3, j = pw < 3 ? (ridx >> 3) & 1 : (ridx >> 2) & 1;
    const int cq = 4 * pw + (pw < 3 ? 2 * (ridx & 1) : 0) + sw, pos = 2 * (g >> 1) + j, b = g & 1;
    const int col = 32 * cq + 8 * le + 2 * pos;
    const unsigned xp = *(const unsigned*)(x + b * D + col);
    const int om = W_MOD + (b * 2688 + col) * 4;
    const int vm[3] = {om + 2 * D * 4, om + 5 * D * 4, om + 4 * D * 4};
    unsigned gr[3];
    sweep8<3, 8>(rw, vm, tag, gr);
    const bool up = lane >= 32;
    float xe = up ? bhi(xp) : blo(xp), ga = up ? bhi(gr[0]) : blo(gr[0]), gb = up ? bhi(gr[1]) : blo(gr[1]), c1 = rbf(1.f + (up ? bhi(gr[2]) : blo(gr[2])));
    int vb[4], vd[4];
    half_line_slots(W_B + ridx * 128 + sw * 64, 64, side, lane, vb);
    half_line_slots(W_D + ridx * 128 + sw * 64, 56, side, lane, vd);
    const int rep = (lane >> 2) & 7;
    int vy = lane < 4 ? (b * D + col) * 2 : OOB;
    asm volatile("" : "+v"(vy));
    if (WARM & 2) __builtin_amdgcn_raw_buffer_store_b32(xp, mkr(out, 2 * D * 2), vy, 0, OUTAUX);
    int vx = (up || le == 3) ? OOB : near(W_X + rep * X_REP + (b * 56 + 4 * cq + pos) * 128 + le * 16, ((rep >> 2) ^ (dec >> (8 + (rep & 3))) ^ (dec >> 4)) & 1);
    asm volatile("" : "+v"(vb[0]), "+v"(vb[1]), "+v"(vb[2]), "+v"(vb[3]), "+v"(vd[0]), "+v"(vd[1]), "+v"(vd[2]), "+v"(vd[3]), "+v"(vx), "+v"(xe), "+v"(ga), "+v"(gb), "+v"(c1));
    BAR(); BAR();
    NAP(DLY_B);
    const float s = reduce_half_line(rw, vb, tag);
    STAMP(18);
    const float x1 = rbf(xe + rbf(ga * rbf(s))), u = x1 * c1;
    const auto uu = __builtin_amdgcn_permlane32_swap(asu(u), asu(u), false, false);
    const unsigned upk = pk(asf(uu[0]), asf(uu[1]));
    float st = tr32(x1, x1 * x1);
    st += dpp<X1>(st); st += dpp<X2>(st);
    const auto ss = __builtin_amdgcn_permlane32_swap(asu(st), asu(st), false, false);
    const unsigned u0 = dppu<0x08>(upk), u1 = dppu<0x5D>(upk);
    st_dev16(rw, vx, le == 2 ? ss[0] : u0, le == 2 ? ss[1] : u1, tag);
    STAMP(19);
    NAP(DLY_D);
    const float s2 = reduce_half_line(rw, vd, tag);
    STAMP(22);
    const float y = x1 + rbf(gb * rbf(s2));
    const auto yy = __builtin_amdgcn_permlane32_swap(asu(y), asu(y), false, false);
    __builtin_amdgcn_raw_buffer_store_b32(pk(asf(yy[0]), asf(yy[1])), mkr(out, 2 * D * 2), vy, 0, OUTAUX);
    if (blockIdx.x == 0 && tid == 256) __builtin_amdgcn_raw_buffer_store_b32(e0 + 1u, rw, W_E, 0, AUX_SC1);
    STAMP(23);
  }
  STAMP_FLUSH();
}

#define WS_BACK 0
extern "C" void kernel_launch(void* const* d_in, const int* in_sizes, int n_in, void* d_out, int out_size, void* d_ws, size_t ws_size,
                              hipStream_t stream) {
  unsigned char* ws = (unsigned char*)d_ws + (WS_BACK ? ((ws_size - W_END) & ~(size_t)0x3fffff) : 0);
  k_block<<<256, 512, 0, stream>>>((const u16*)d_in[0], (const u16*)d_in[1], (const u16*)d_in[2], (const u16*)d_in[3], (const u16*)d_in[4],
                                   (const u16*)d_in[6], (const u16*)d_in[7], (const u16*)d_in[8], (const u16*)d_in[9], (const u16*)d_in[10],
                                   (u16*)d_out, ws
                                   );
}
```
